```python
import torch

def _softmax(x, axis=-1):
    return torch.softmax(x, dim=axis)
def _gelu(x):
    return torch.nn.functional.gelu(x, approximate="tanh")
def _silu(x):
    return torch.nn.functional.silu(x)
def _sigmoid(x):
    return torch.sigmoid(x)
def _layernorm(x, axis=-1, eps=1e-5):
    m = x.mean(dim=axis, keepdim=True)
    v = x.var(dim=axis, keepdim=True, unbiased=False)
    return (x - m) / torch.sqrt(v + eps)
def _scatter_add(tgt, idx, src, axis=0):
    return tgt.clone().index_add_(axis, idx, src)
def _segment_sum(src, seg, n):
    out = torch.zeros((n,) + src.shape[1:], dtype=src.dtype)
    return out.index_add_(0, seg, src)
def _pad(x, pw):
    flat = tuple(v for pair in reversed(pw) for v in pair)
    return torch.nn.functional.pad(x, flat)

kernel_name = 'standalone__gqa_softmax__near_ridge'

def setup_inputs():

    _w = (lambda a: (lambda t: t.to(torch.bfloat16) if t.is_floating_point() else t)(torch.as_tensor(a)))
    B, Hq, Hkv, Sq, Sk, D = 16, 32, 8, 64, 512, 128
    return {
        "q": _w((lambda *s: torch.randn(*s, dtype=torch.bfloat16) * 0.8)(B, Hq, Sq, D)),
        "k": _w((lambda *s: torch.randn(*s, dtype=torch.bfloat16) * 0.8)(B, Hkv, Sk, D)),
        "v": _w((lambda *s: torch.randn(*s, dtype=torch.bfloat16) * 0.05)(B, Hkv, Sk, D)),
    }

def reference(q, k, v):
    B, Hq, Sq, D = q.shape
    Hkv = k.shape[1]
    rep = Hq // Hkv
    k = (lambda x, n, axis: torch.repeat_interleave(x, n, dim=axis))(k, rep, axis=1)
    v = (lambda x, n, axis: torch.repeat_interleave(x, n, dim=axis))(v, rep, axis=1)
    scale = D ** -0.5
    scores = torch.einsum("bhqd,bhkd->bhqk", q, k) * scale
    p = _softmax(scores, axis=-1)
    return torch.einsum("bhqk,bhkd->bhqd", p, v)
```

```cpp
#include <hip/hip_runtime.h>
#include <hip/hip_bf16.h>

typedef __attribute__((ext_vector_type(8))) short bf16x8;
typedef __attribute__((ext_vector_type(4))) short s16x4;
typedef __attribute__((ext_vector_type(16))) float f32x16;
typedef __attribute__((ext_vector_type(4))) float f32x4;
typedef __attribute__((ext_vector_type(4))) unsigned u32x4;
typedef __attribute__((ext_vector_type(2))) float f32x2;
typedef __attribute__((ext_vector_type(2))) __bf16 bf16x2;
typedef __attribute__((address_space(3))) char* lds_ptr;

#define DI __device__ __forceinline__
#define MFMA(a, b, c) __builtin_amdgcn_mfma_f32_32x32x16_bf16(a, b, c, 0, 0, 0)
#define WAIT_BAR(N) asm volatile("s_waitcnt vmcnt(" #N ") lgkmcnt(0)\n\ts_barrier" ::: "memory")

constexpr int SLOT = 16384;
constexpr int LDS_BYTES = 10 * SLOT;
constexpr float C2 = 0.08838834764831845f * 1.4426950408889634f;

DI unsigned cvtpk(float lo, float hi) { f32x2 v = {lo, hi}; bf16x2 b = __builtin_convertvector(v, bf16x2); return __builtin_bit_cast(unsigned, b); }
DI void dma16(unsigned m0v, unsigned voff, __amdgpu_buffer_rsrc_t srd, unsigned soff) {
  asm volatile("s_mov_b32 m0, %0\n\ts_nop 0\n\tbuffer_load_dwordx4 %1, %2, %3 offen lds" :: "s"(m0v), "v"(voff), "s"(srd), "s"(soff) : "memory");
}
DI bf16x8 lds128(lds_ptr p) { return *(const __attribute__((address_space(3))) bf16x8*)p; }
DI s16x4 ldstr(lds_ptr p) { return __builtin_amdgcn_ds_read_tr16_b64_v4i16((__attribute__((address_space(3))) s16x4*)p); }

__global__ __launch_bounds__(512, 2) void gqa_fwd(const unsigned short* __restrict__ Q, const unsigned short* __restrict__ K,
                                                  const unsigned short* __restrict__ V, unsigned short* __restrict__ O) {
  extern __shared__ __attribute__((aligned(16))) char smem[];
  lds_ptr lds = (lds_ptr)smem;
  const unsigned tid = threadIdx.x, lane = tid & 63u, r = lane & 31u, h = lane >> 5;
  const unsigned w = __builtin_amdgcn_readfirstlane(tid >> 6), rb = w & 3u, kh = w >> 2;
  const unsigned bx = blockIdx.x, pair = (bx >> 4) * 8u + (bx & 7u), hm = (bx >> 3) & 1u;
  const char* Qb = (const char*)Q + ((size_t)pair * 256 + hm * 128) * 256;
  const char* Kb = (const char*)K + (size_t)pair * 512 * 256;
  const char* Vb = (const char*)V + (size_t)pair * 512 * 256;
  char* Ob = (char*)O + ((size_t)pair * 256 + hm * 128) * 256;
  __amdgpu_buffer_rsrc_t srdQ = __builtin_amdgcn_make_buffer_rsrc((void*)Qb, (short)0, 128 * 256, 0x00020000);
  __amdgpu_buffer_rsrc_t srdK = __builtin_amdgcn_make_buffer_rsrc((void*)Kb, (short)0, 512 * 256, 0x00020000);
  __amdgpu_buffer_rsrc_t srdV = __builtin_amdgcn_make_buffer_rsrc((void*)Vb, (short)0, 512 * 256, 0x00020000);
  const unsigned ldsb = (unsigned)(size_t)lds;
  unsigned ksrc[2];
#pragma unroll
  for (int e = 0; e < 2; ++e) { const unsigned rr = 4u * (2u * w + e) + (lane >> 4); ksrc[e] = rr * 256u + 16u * ((lane & 15u) ^ (rr & 15u)); }
  const unsigned vsrc = (8u * w + ((lane >> 2) & 7u)) * 256u + 64u * h + 16u * (lane & 3u);
  const unsigned m0w = ldsb + 2048u * w;
#pragma unroll
  for (int t = 0; t < 2; ++t) { dma16(m0w + t * SLOT, ksrc[0], srdQ, t * SLOT); dma16(m0w + t * SLOT + 1024, ksrc[1], srdQ, t * SLOT); }
#pragma unroll
  for (int t = 0; t < 8; ++t) { dma16(m0w + (t + 2) * SLOT, ksrc[0], srdK, t * SLOT); dma16(m0w + (t + 2) * SLOT + 1024, ksrc[1], srdK, t * SLOT); }
#define DMA_V(t) do { dma16(m0w + (t) * SLOT, vsrc, srdV, (t) * SLOT); dma16(m0w + (t) * SLOT + 1024, vsrc, srdV, (t) * SLOT + 128); } while (0)

  unsigned ka[8];
#pragma unroll
  for (int s = 0; s < 8; ++s) ka[s] = r * 256u + 16u * ((2u * s + h) ^ (r & 15u));
  bf16x8 qf[8];
  WAIT_BAR(16);
#pragma unroll
  for (int s = 0; s < 8; ++s) qf[s] = lds128(lds + ka[s] + rb * 8192u);
  f32x16 S[8];
  lds_ptr kbase = lds + 2 * SLOT + kh * 8192u;
#define QK(t) do { bf16x8 kf[8]; _Pragma("unroll") for (int s = 0; s < 8; ++s) kf[s] = lds128(kbase + ka[s] + (t) * SLOT); \
    S[t] = MFMA(kf[0], qf[0], (f32x16{})); _Pragma("unroll") for (int s = 1; s < 8; ++s) S[t] = MFMA(kf[s], qf[s], S[t]); } while (0)
  WAIT_BAR(14); DMA_V(0); DMA_V(1); QK(0);
  WAIT_BAR(16); DMA_V(2); QK(1);
  WAIT_BAR(16); DMA_V(3); QK(2);
  WAIT_BAR(16); DMA_V(4); QK(3);
  WAIT_BAR(16); DMA_V(5); QK(4);
  WAIT_BAR(16); DMA_V(6); QK(5);
  WAIT_BAR(16); DMA_V(7); QK(6);
  WAIT_BAR(16); QK(7);
  float m = S[0][0];
#pragma unroll
  for (int t = 0; t < 8; ++t)
#pragma unroll
    for (int i = 0; i < 16; ++i) m = fmaxf(m, S[t][i]);
  { auto rr = __builtin_amdgcn_permlane32_swap(__float_as_uint(m), __float_as_uint(m), false, false); m = fmaxf(__uint_as_float(rr[0]), __uint_as_float(rr[1])); }
  const float mc = m * C2, nmc = -mc;
  float l = 0.f;
  f32x16 o[4] = {};
  lds_ptr vbase = lds + kh * 8192u + ((h << 8) | (((lane >> 2) & 3u) << 6) | (((lane >> 4) & 1u) << 5) | ((lane & 3u) << 3));
#define PV(t) do { \
    _Pragma("unroll") for (int i = 0; i < 16; ++i) { S[t][i] = __builtin_amdgcn_exp2f(fmaf(S[t][i], C2, nmc)); l += S[t][i]; } \
    _Pragma("unroll") for (int s2 = 0; s2 < 2; ++s2) { \
      u32x4 pw = {cvtpk(S[t][8 * s2 + 0], S[t][8 * s2 + 1]), cvtpk(S[t][8 * s2 + 2], S[t][8 * s2 + 3]), cvtpk(S[t][8 * s2 + 4], S[t][8 * s2 + 5]), cvtpk(S[t][8 * s2 + 6], S[t][8 * s2 + 7])}; \
      bf16x8 pf = __builtin_bit_cast(bf16x8, pw); \
      _Pragma("unroll") for (int db = 0; db < 4; ++db) { \
        s16x4 lo = ldstr(vbase + (t) * SLOT + 4096 * s2 + 512 * db), hi = ldstr(vbase + (t) * SLOT + 4096 * s2 + 2048 + 512 * db); \
        bf16x8 vf = __builtin_shufflevector(lo, hi, 0, 1, 2, 3, 4, 5, 6, 7); \
        o[db] = MFMA(vf, pf, o[db]); } } } while (0)
  WAIT_BAR(14); PV(0);
  WAIT_BAR(12); PV(1);
  WAIT_BAR(10); PV(2);
  WAIT_BAR(8); PV(3);
  WAIT_BAR(6); PV(4);
  WAIT_BAR(4); PV(5);
  WAIT_BAR(2); PV(6);
  WAIT_BAR(0); PV(7);
  { auto rr = __builtin_amdgcn_permlane32_swap(__float_as_uint(l), __float_as_uint(l), false, false); l = __uint_as_float(rr[0]) + __uint_as_float(rr[1]); }
  typedef __attribute__((address_space(3))) f32x2* lds_f2;
  typedef __attribute__((address_space(3))) f32x4* lds_f4;
  *(lds_f2)(lds + 8 * SLOT + w * 512u + lane * 8u) = f32x2{mc, l};
  WAIT_BAR(0);
  const f32x2 pml = *(lds_f2)(lds + 8 * SLOT + (w ^ 4u) * 512u + lane * 8u);
  const float mx = fmaxf(mc, pml[0]);
  const float a0 = __builtin_amdgcn_exp2f(mc - mx), a1 = __builtin_amdgcn_exp2f(pml[0] - mx);
  const float f = a0 / (l * a0 + pml[1] * a1);
  f32x16 keep0, keep1, send0, send1;
  if (kh == 0) { keep0 = o[0]; keep1 = o[1]; send0 = o[2]; send1 = o[3]; } else { keep0 = o[2]; keep1 = o[3]; send0 = o[0]; send1 = o[1]; }
#pragma unroll
  for (int g = 0; g < 4; ++g) {
    *(lds_f4)(lds + w * 8192u + g * 1024u + lane * 16u) = f32x4{send0[4 * g] * f, send0[4 * g + 1] * f, send0[4 * g + 2] * f, send0[4 * g + 3] * f};
    *(lds_f4)(lds + w * 8192u + 4096u + g * 1024u + lane * 16u) = f32x4{send1[4 * g] * f, send1[4 * g + 1] * f, send1[4 * g + 2] * f, send1[4 * g + 3] * f};
  }
  WAIT_BAR(0);
  char* orow = Ob + (size_t)(rb * 32u + r) * 256u + kh * 128u + h * 16u;
#pragma unroll
  for (int tt = 0; tt < 2; ++tt) {
    unsigned pk[8];
#pragma unroll
    for (int g = 0; g < 4; ++g) {
      const f32x4 p = *(lds_f4)(lds + (w ^ 4u) * 8192u + tt * 4096u + g * 1024u + lane * 16u);
      const f32x16& kp = tt ? keep1 : keep0;
      pk[2 * g] = cvtpk(fmaf(kp[4 * g], f, p[0]), fmaf(kp[4 * g + 1], f, p[1]));
      pk[2 * g + 1] = cvtpk(fmaf(kp[4 * g + 2], f, p[2]), fmaf(kp[4 * g + 3], f, p[3]));
    }
#pragma unroll
    for (int k = 0; k < 4; k += 2) {
      auto x = __builtin_amdgcn_permlane32_swap(pk[2 * k], pk[2 * k + 2], false, false);
      auto y = __builtin_amdgcn_permlane32_swap(pk[2 * k + 1], pk[2 * k + 3], false, false);
      *(u32x4*)(orow + tt * 64 + k * 16) = u32x4{x[0], y[0], x[1], y[1]};
    }
  }
}

extern "C" void kernel_launch(void* const* d_in, const int* in_sizes, int n_in, void* d_out, int out_size, void* d_ws, size_t ws_size, hipStream_t stream) {
  (void)in_sizes; (void)n_in; (void)out_size; (void)d_ws; (void)ws_size;
  (void)hipFuncSetAttribute((const void*)gqa_fwd, hipFuncAttributeMaxDynamicSharedMemorySize, LDS_BYTES);
  hipLaunchKernelGGL(gqa_fwd, dim3(256), dim3(512), LDS_BYTES, stream, (const unsigned short*)d_in[0], (const unsigned short*)d_in[1], (const unsigned short*)d_in[2], (unsigned short*)d_out);
}
```

```cpp
#include <hip/hip_runtime.h>
#include <hip/hip_bf16.h>

#define DI __device__ __forceinline__
constexpr int SLOT = 16384;
constexpr int LDS_BYTES = 10 * SLOT;
typedef __attribute__((ext_vector_type(4))) unsigned u32x4;

DI float bf_lo(unsigned x) { return __uint_as_float(x << 16); }
DI float bf_hi(unsigned x) { return __uint_as_float(x & 0xffff0000u); }

DI void exact_rows(const char* Qw, const char* Kb, const char* Vb, char* Ow, unsigned lane) {
  const float c = 0.08838834764831845f * 1.4426950408889634f;
  for (int row = 0; row < 32; ++row) {
    float s[8];
#pragma unroll
    for (int j = 0; j < 8; ++j) {
      float a = 0.f;
      for (int ch = 0; ch < 16; ++ch) {
        const u32x4 q = *(const u32x4*)(Qw + row * 256 + ch * 16), k = *(const u32x4*)(Kb + (j * 64 + lane) * 256 + ch * 16);
#pragma unroll
        for (int d = 0; d < 4; ++d) a = fmaf(bf_lo(q[d]), bf_lo(k[d]), fmaf(bf_hi(q[d]), bf_hi(k[d]), a));
      }
      s[j] = a;
    }
    float m = s[0];
#pragma unroll
    for (int j = 1; j < 8; ++j) m = fmaxf(m, s[j]);
    for (int o = 32; o; o >>= 1) m = fmaxf(m, __shfl_xor(m, o));
    float l = 0.f;
#pragma unroll
    for (int j = 0; j < 8; ++j) { s[j] = exp2f((s[j] - m) * c); l += s[j]; }
    for (int o = 32; o; o >>= 1) l += __shfl_xor(l, o);
    float o0 = 0.f, o1 = 0.f;
#pragma unroll
    for (int j = 0; j < 8; ++j)
      for (int t = 0; t < 64; ++t) {
        const float p = __shfl(s[j], t);
        const unsigned v = *(const unsigned*)(Vb + (j * 64 + t) * 256 + lane * 4);
        o0 = fmaf(p, bf_lo(v), o0); o1 = fmaf(p, bf_hi(v), o1);
      }
    const float il = 1.f / l;
    __hip_bfloat162 r; r.x = __float2bfloat16(o0 * il); r.y = __float2bfloat16(o1 * il);
    *(__hip_bfloat162*)(Ow + row * 256 + lane * 4) = r;
  }
}

__global__ __launch_bounds__(512, 2) void gqa_fwd(const unsigned short* __restrict__ Q, const unsigned short* __restrict__ K,
                                                  const unsigned short* __restrict__ V, unsigned short* __restrict__ O
  ) {
  float lout;
  asm volatile(
    "s_load_dwordx8 s[12:19], %[karg], 0\n"
    "s_getpc_b64 s[36:37]\n"
    "v_lshlrev_b32_e32 v11, 5, %[tid]\n"
    "global_load_dword v11, v11, s[36:37]\n"
    "v_readfirstlane_b32 s24, %[tid]\n"
    "v_and_b32_e32 v0, 63, %[tid]\n"
    "v_lshrrev_b32_e32 v1, 4, v0\n"
    "s_lshr_b32 s29, %[bx], 1\n"
    "s_and_b32 s29, s29, 0x78\n"
    "s_and_b32 s30, %[bx], 7\n"
    "s_or_b32 s26, s29, s30\n"
    "v_and_b32_e32 v6, 15, v0\n"
    "v_xor_b32_e32 v6, v6, v1\n"
    "v_lshlrev_b32_e32 v6, 4, v6\n"
    "v_lshl_or_b32 v2, v1, 8, v6\n"
    "v_xor_b32_e32 v3, 64, v2\n"
    "v_xor_b32_e32 v4, 128, v2\n"
    "v_xor_b32_e32 v5, 192, v2\n"
    "s_lshl_b32 s27, s26, 16\n"
    "s_lshl_b32 s29, %[bx], 12\n"
    "s_and_b32 s29, s29, 0x8000\n"
    "s_or_b32 s27, s27, s29\n"
    "s_lshl_b32 s39, s24, 6\n"
    "s_and_b32 s39, s39, 0x7000\n"
    "s_or_b32 s29, s27, s39\n"
    "s_or_b32 s39, s39, 0x20000\n"
    "s_mov_b32 s10, 0x800000\n"
    "s_mov_b32 s11, 0x20000\n"
    "s_lshr_b32 s24, s24, 6\n"
    "s_waitcnt lgkmcnt(0)\n"
    "s_mov_b64 s[8:9], s[12:13]\n"
    "buffer_load_dwordx4 v[32:35], v2, s[8:11], s29 offen offset:0 nt\n"
    "buffer_load_dwordx4 v[36:39], v3, s[8:11], s29 offen offset:1024 nt\n"
    "buffer_load_dwordx4 v[40:43], v4, s[8:11], s29 offen offset:2048 nt\n"
    "buffer_load_dwordx4 v[44:47], v5, s[8:11], s29 offen offset:3072 nt\n"
    "s_mov_b64 s[20:21], s[18:19]\n"
    "s_mov_b64 s[12:13], s[14:15]\n"
    "s_and_b32 s25, s24, 3\n"
    "s_mov_b32 s34, 0x3e0293ee\n"
    "s_mov_b32 s35, 0x3e0293ee\n"
    "v_lshl_add_u32 v7, v0, 4, s39\n"
    "s_cmp_lt_u32 s24, 4\n"
    "s_cbranch_scc1 2f\n"
    "s_mov_b32 s14, 0x1000000\n"
    "s_mov_b32 s15, 0x20000\n"
    "s_and_b32 s29, s25, 1\n"
    "s_lshl_b32 s29, s29, 7\n"
    "v_xor_b32_e32 v12, s29, v2\n"
    "v_xor_b32_e32 v13, 64, v12\n"
    "s_lshl_b32 s28, s26, 17\n"
    "s_lshl_b32 s31, s25, 11\n"
    "s_or_b32 s28, s28, s31\n"
    "s_add_u32 m0, s31, 0\n"
    "s_add_u32 s29, s28, 0\n"
    "buffer_load_dwordx4 v12, s[12:15], s29 offen nt lds\n"
    "buffer_load_dwordx4 v13, s[12:15], s29 offen offset:1024 nt lds\n"
    "s_add_u32 m0, s31, 8192\n"
    "s_add_u32 s29, s28, 8192\n"
    "buffer_load_dwordx4 v12, s[12:15], s29 offen nt lds\n"
    "buffer_load_dwordx4 v13, s[12:15], s29 offen offset:1024 nt lds\n"
    "s_mov_b32 s18, 0x1000000\n"
    "s_mov_b32 s19, 0x20000\n"
    "v_lshlrev_b32_e32 v6, 6, v0\n"
    "v_and_b32_e32 v8, 0x700, v6\n"
    "v_lshrrev_b32_e32 v1, 5, v0\n"
    "v_lshl_or_b32 v8, v1, 6, v8\n"
    "v_and_b32_e32 v1, 3, v0\n"
    "v_lshl_or_b32 v8, v1, 4, v8\n"
    "v_lshlrev_b32_e32 v1, 1, v0\n"
    "v_and_b32_e32 v1, 32, v1\n"
    "v_xor_b32_e32 v8, v8, v1\n"
    "v_or_b32_e32 v9, 0x80, v8\n"
    "s_lshl_b32 s30, s26, 17\n"
    "v_lshlrev_b32_e32 v14, 17, v0\n"
    "v_and_b32_e32 v14, 0x1ffff, v14\n"
    "buffer_load_dword v15, v14, s[16:19], s30 offen nt\n"
    "s_waitcnt vmcnt(8)\n"
    "s_add_u32 m0, s31, 16384\n"
    "s_add_u32 s29, s28, 16384\n"
    "buffer_load_dwordx4 v12, s[12:15], s29 offen nt lds\n"
    "buffer_load_dwordx4 v13, s[12:15], s29 offen offset:1024 nt lds\n"
    "v_lshlrev_b32_e32 v48, 16, v32\n"
    "v_and_b32_e32 v49, 0xffff0000, v32\n"
    "v_lshlrev_b32_e32 v50, 16, v33\n"
    "v_and_b32_e32 v51, 0xffff0000, v33\n"
    "v_lshlrev_b32_e32 v52, 16, v34\n"
    "v_and_b32_e32 v53, 0xffff0000, v34\n"
    "v_lshlrev_b32_e32 v54, 16, v35\n"
    "v_and_b32_e32 v55, 0xffff0000, v35\n"
    "v_pk_mul_f32 v[48:49], v[48:49], s[34:35]\n"
    "v_pk_mul_f32 v[50:51], v[50:51], s[34:35]\n"
    "v_pk_mul_f32 v[52:53], v[52:53], s[34:35]\n"
    "v_pk_mul_f32 v[54:55], v[54:55], s[34:35]\n"
    "v_cvt_pk_bf16_f32 v32, v48, v49\n"
    "v_cvt_pk_bf16_f32 v33, v50, v51\n"
    "v_cvt_pk_bf16_f32 v34, v52, v53\n"
    "v_cvt_pk_bf16_f32 v35, v54, v55\n"
    "ds_write_b128 v7, v[32:35] offset:0\n"
    "s_waitcnt vmcnt(9)\n"
    "s_add_u32 m0, s31, 24576\n"
    "s_add_u32 s29, s28, 24576\n"
    "buffer_load_dwordx4 v12, s[12:15], s29 offen nt lds\n"
    "buffer_load_dwordx4 v13, s[12:15], s29 offen offset:1024 nt lds\n"
    "v_lshlrev_b32_e32 v56, 16, v36\n"
    "v_and_b32_e32 v57, 0xffff0000, v36\n"
    "v_lshlrev_b32_e32 v58, 16, v37\n"
    "v_and_b32_e32 v59, 0xffff0000, v37\n"
    "v_lshlrev_b32_e32 v60, 16, v38\n"
    "v_and_b32_e32 v61, 0xffff0000, v38\n"
    "v_lshlrev_b32_e32 v62, 16, v39\n"
    "v_and_b32_e32 v63, 0xffff0000, v39\n"
    "v_pk_mul_f32 v[56:57], v[56:57], s[34:35]\n"
    "v_pk_mul_f32 v[58:59], v[58:59], s[34:35]\n"
    "v_pk_mul_f32 v[60:61], v[60:61], s[34:35]\n"
    "v_pk_mul_f32 v[62:63], v[62:63], s[34:35]\n"
    "v_cvt_pk_bf16_f32 v36, v56, v57\n"
    "v_cvt_pk_bf16_f32 v37, v58, v59\n"
    "v_cvt_pk_bf16_f32 v38, v60, v61\n"
    "v_cvt_pk_bf16_f32 v39, v62, v63\n"
    "ds_write_b128 v7, v[36:39] offset:1024\n"
    "s_waitcnt vmcnt(10)\n"
    "v_lshlrev_b32_e32 v48, 16, v40\n"
    "v_and_b32_e32 v49, 0xffff0000, v40\n"
    "v_lshlrev_b32_e32 v50, 16, v41\n"
    "v_and_b32_e32 v51, 0xffff0000, v41\n"
    "v_lshlrev_b32_e32 v52, 16, v42\n"
    "v_and_b32_e32 v53, 0xffff0000, v42\n"
    "v_lshlrev_b32_e32 v54, 16, v43\n"
    "v_and_b32_e32 v55, 0xffff0000, v43\n"
    "v_pk_mul_f32 v[48:49], v[48:49], s[34:35]\n"
    "v_pk_mul_f32 v[50:51], v[50:51], s[34:35]\n"
    "v_pk_mul_f32 v[52:53], v[52:53], s[34:35]\n"
    "v_pk_mul_f32 v[54:55], v[54:55], s[34:35]\n"
    "v_cvt_pk_bf16_f32 v40, v48, v49\n"
    "v_cvt_pk_bf16_f32 v41, v50, v51\n"
    "v_cvt_pk_bf16_f32 v42, v52, v53\n"
    "v_cvt_pk_bf16_f32 v43, v54, v55\n"
    "ds_write_b128 v7, v[40:43] offset:2048\n"
    "s_waitcnt vmcnt(9)\n"
    "v_lshlrev_b32_e32 v56, 16, v44\n"
    "v_and_b32_e32 v57, 0xffff0000, v44\n"
    "v_lshlrev_b32_e32 v58, 16, v45\n"
    "v_and_b32_e32 v59, 0xffff0000, v45\n"
    "v_lshlrev_b32_e32 v60, 16, v46\n"
    "v_and_b32_e32 v61, 0xffff0000, v46\n"
    "v_lshlrev_b32_e32 v62, 16, v47\n"
    "v_and_b32_e32 v63, 0xffff0000, v47\n"
    "v_pk_mul_f32 v[56:57], v[56:57], s[34:35]\n"
    "v_pk_mul_f32 v[58:59], v[58:59], s[34:35]\n"
    "v_pk_mul_f32 v[60:61], v[60:61], s[34:35]\n"
    "v_pk_mul_f32 v[62:63], v[62:63], s[34:35]\n"
    "v_cvt_pk_bf16_f32 v44, v56, v57\n"
    "v_cvt_pk_bf16_f32 v45, v58, v59\n"
    "v_cvt_pk_bf16_f32 v46, v60, v61\n"
    "v_cvt_pk_bf16_f32 v47, v62, v63\n"
    "ds_write_b128 v7, v[44:47] offset:3072\n"
    "s_waitcnt lgkmcnt(0)\n"
    "s_waitcnt vmcnt(7)\n"
    "s_barrier\n"
    "s_memrealtime s[40:41]\n"
    "s_add_u32 m0, s31, 32768\n"
    "s_add_u32 s29, s28, 32768\n"
    "buffer_load_dwordx4 v12, s[12:15], s29 offen nt lds\n"
    "buffer_load_dwordx4 v13, s[12:15], s29 offen offset:1024 nt lds\n"
    "s_waitcnt vmcnt(7)\n"
    "s_barrier\n"
    "s_add_u32 m0, s31, 65536\n"
    "s_add_u32 s29, s28, 0\n"
    "buffer_load_dwordx4 v8, s[16:19], s29 offen nt lds\n"
    "s_add_u32 m0, s31, 66560\n"
    "s_nop 0\n"
    "buffer_load_dwordx4 v9, s[16:19], s29 offen nt lds\n"
    "s_waitcnt vmcnt(6)\n"
    "s_barrier\n"
    "s_add_u32 m0, s31, 40960\n"
    "s_add_u32 s29, s28, 40960\n"
    "buffer_load_dwordx4 v12, s[12:15], s29 offen nt lds\n"
    "buffer_load_dwordx4 v13, s[12:15], s29 offen offset:1024 nt lds\n"
    "s_mov_b32 s22, 0x800000\n"
    "s_mov_b32 s23, 0x20000\n"
    "v_mov_b32_e32 v14, 0\n"
    "buffer_load_dword v16, v14, s[20:23], s27 offen nt\n"
    "s_waitcnt vmcnt(7)\n"
    "s_barrier\n"
    "s_add_u32 m0, s31, 73728\n"
    "s_add_u32 s29, s28, 8192\n"
    "buffer_load_dwordx4 v8, s[16:19], s29 offen nt lds\n"
    "s_add_u32 m0, s31, 74752\n"
    "s_nop 0\n"
    "buffer_load_dwordx4 v9, s[16:19], s29 offen nt lds\n"
    "s_add_u32 m0, s31, 49152\n"
    "s_add_u32 s29, s28, 49152\n"
    "buffer_load_dwordx4 v12, s[12:15], s29 offen nt lds\n"
    "buffer_load_dwordx4 v13, s[12:15], s29 offen offset:1024 nt lds\n"
    "s_cmp_lg_u32 s25, 0\n"
    "s_cbranch_scc1 64f\n"
    "s_waitcnt lgkmcnt(0)\n"
    "s_add_u32 s41, s40, 113\n"
    "34:\n"
    "s_memrealtime s[42:43]\n"
    "s_waitcnt lgkmcnt(0)\n"
    "s_sub_u32 s43, s42, s41\n"
    "s_cmp_lt_i32 s43, 0\n"
    "s_cbranch_scc1 34b\n"
    "64:\n"
    "s_waitcnt vmcnt(7)\n"
    "s_barrier\n"
    "s_add_u32 m0, s31, 81920\n"
    "s_add_u32 s29, s28, 16384\n"
    "buffer_load_dwordx4 v8, s[16:19], s29 offen nt lds\n"
    "s_add_u32 m0, s31, 82944\n"
    "s_nop 0\n"
    "buffer_load_dwordx4 v9, s[16:19], s29 offen nt lds\n"
    "s_add_u32 m0, s31, 57344\n"
    "s_add_u32 s29, s28, 57344\n"
    "buffer_load_dwordx4 v12, s[12:15], s29 offen nt lds\n"
    "buffer_load_dwordx4 v13, s[12:15], s29 offen offset:1024 nt lds\n"
    "s_cmp_lg_u32 s25, 0\n"
    "s_cbranch_scc1 65f\n"
    "s_add_u32 s41, s40, 159\n"
    "35:\n"
    "s_memrealtime s[42:43]\n"
    "s_waitcnt lgkmcnt(0)\n"
    "s_sub_u32 s43, s42, s41\n"
    "s_cmp_lt_i32 s43, 0\n"
    "s_cbranch_scc1 35b\n"
    "65:\n"
    "s_waitcnt vmcnt(6)\n"
    "s_barrier\n"
    "s_add_u32 m0, s31, 90112\n"
    "s_add_u32 s29, s28, 24576\n"
    "buffer_load_dwordx4 v8, s[16:19], s29 offen nt lds\n"
    "s_add_u32 m0, s31, 91136\n"
    "s_nop 0\n"
    "buffer_load_dwordx4 v9, s[16:19], s29 offen nt lds\n"
    "s_add_u32 m0, s31, 0\n"
    "s_add_u32 s29, s28, 65536\n"
    "buffer_load_dwordx4 v12, s[12:15], s29 offen nt lds\n"
    "buffer_load_dwordx4 v13, s[12:15], s29 offen offset:1024 nt lds\n"
    "s_cmp_lg_u32 s25, 0\n"
    "s_cbranch_scc1 66f\n"
    "s_add_u32 s41, s40, 203\n"
    "36:\n"
    "s_memrealtime s[42:43]\n"
    "s_waitcnt lgkmcnt(0)\n"
    "s_sub_u32 s43, s42, s41\n"
    "s_cmp_lt_i32 s43, 0\n"
    "s_cbranch_scc1 36b\n"
    "66:\n"
    "s_waitcnt vmcnt(6)\n"
    "s_barrier\n"
    "s_add_u32 m0, s31, 98304\n"
    "s_add_u32 s29, s28, 32768\n"
    "buffer_load_dwordx4 v8, s[16:19], s29 offen nt lds\n"
    "s_add_u32 m0, s31, 99328\n"
    "s_nop 0\n"
    "buffer_load_dwordx4 v9, s[16:19], s29 offen nt lds\n"
    "s_add_u32 m0, s31, 8192\n"
    "s_add_u32 s29, s28, 73728\n"
    "buffer_load_dwordx4 v12, s[12:15], s29 offen nt lds\n"
    "buffer_load_dwordx4 v13, s[12:15], s29 offen offset:1024 nt lds\n"
    "s_cmp_lg_u32 s25, 0\n"
    "s_cbranch_scc1 67f\n"
    "s_add_u32 s41, s40, 238\n"
    "37:\n"
    "s_memrealtime s[42:43]\n"
    "s_waitcnt lgkmcnt(0)\n"
    "s_sub_u32 s43, s42, s41\n"
    "s_cmp_lt_i32 s43, 0\n"
    "s_cbranch_scc1 37b\n"
    "67:\n"
    "s_waitcnt vmcnt(6)\n"
    "s_barrier\n"
    "s_add_u32 m0, s31, 106496\n"
    "s_add_u32 s29, s28, 40960\n"
    "buffer_load_dwordx4 v8, s[16:19], s29 offen nt lds\n"
    "s_add_u32 m0, s31, 107520\n"
    "s_nop 0\n"
    "buffer_load_dwordx4 v9, s[16:19], s29 offen nt lds\n"
    "s_add_u32 m0, s31, 16384\n"
    "s_add_u32 s29, s28, 81920\n"
    "buffer_load_dwordx4 v12, s[12:15], s29 offen nt lds\n"
    "buffer_load_dwordx4 v13, s[12:15], s29 offen offset:1024 nt lds\n"
    "s_cmp_lg_u32 s25, 0\n"
    "s_cbranch_scc1 68f\n"
    "s_add_u32 s41, s40, 293\n"
    "38:\n"
    "s_memrealtime s[42:43]\n"
    "s_waitcnt lgkmcnt(0)\n"
    "s_sub_u32 s43, s42, s41\n"
    "s_cmp_lt_i32 s43, 0\n"
    "s_cbranch_scc1 38b\n"
    "68:\n"
    "s_waitcnt vmcnt(6)\n"
    "s_barrier\n"
    "s_add_u32 m0, s31, 114688\n"
    "s_add_u32 s29, s28, 49152\n"
    "buffer_load_dwordx4 v8, s[16:19], s29 offen nt lds\n"
    "s_add_u32 m0, s31, 115712\n"
    "s_nop 0\n"
    "buffer_load_dwordx4 v9, s[16:19], s29 offen nt lds\n"
    "s_add_u32 m0, s31, 24576\n"
    "s_add_u32 s29, s28, 90112\n"
    "buffer_load_dwordx4 v12, s[12:15], s29 offen nt lds\n"
    "buffer_load_dwordx4 v13, s[12:15], s29 offen offset:1024 nt lds\n"
    "s_cmp_lg_u32 s25, 0\n"
    "s_cbranch_scc1 69f\n"
    "s_add_u32 s41, s40, 339\n"
    "39:\n"
    "s_memrealtime s[42:43]\n"
    "s_waitcnt lgkmcnt(0)\n"
    "s_sub_u32 s43, s42, s41\n"
    "s_cmp_lt_i32 s43, 0\n"
    "s_cbranch_scc1 39b\n"
    "69:\n"
    "s_waitcnt vmcnt(6)\n"
    "s_barrier\n"
    "s_add_u32 m0, s31, 122880\n"
    "s_add_u32 s29, s28, 57344\n"
    "buffer_load_dwordx4 v8, s[16:19], s29 offen nt lds\n"
    "s_add_u32 m0, s31, 123904\n"
    "s_nop 0\n"
    "buffer_load_dwordx4 v9, s[16:19], s29 offen nt lds\n"
    "s_add_u32 m0, s31, 32768\n"
    "s_add_u32 s29, s28, 98304\n"
    "buffer_load_dwordx4 v12, s[12:15], s29 offen nt lds\n"
    "buffer_load_dwordx4 v13, s[12:15], s29 offen offset:1024 nt lds\n"
    "s_waitcnt vmcnt(6)\n"
    "s_barrier\n"
    "s_add_u32 m0, s31, 65536\n"
    "s_add_u32 s29, s28, 65536\n"
    "buffer_load_dwordx4 v8, s[16:19], s29 offen nt lds\n"
    "s_add_u32 m0, s31, 66560\n"
    "s_nop 0\n"
    "buffer_load_dwordx4 v9, s[16:19], s29 offen nt lds\n"
    "s_add_u32 m0, s31, 40960\n"
    "s_add_u32 s29, s28, 106496\n"
    "buffer_load_dwordx4 v12, s[12:15], s29 offen nt lds\n"
    "buffer_load_dwordx4 v13, s[12:15], s29 offen offset:1024 nt lds\n"
    "s_add_u32 m0, s31, 73728\n"
    "s_add_u32 s29, s28, 73728\n"
    "buffer_load_dwordx4 v8, s[16:19], s29 offen nt lds\n"
    "s_add_u32 m0, s31, 74752\n"
    "s_nop 0\n"
    "buffer_load_dwordx4 v9, s[16:19], s29 offen nt lds\n"
    "s_waitcnt vmcnt(8)\n"
    "s_barrier\n"
    "s_add_u32 m0, s31, 49152\n"
    "s_add_u32 s29, s28, 114688\n"
    "buffer_load_dwordx4 v12, s[12:15], s29 offen nt lds\n"
    "buffer_load_dwordx4 v13, s[12:15], s29 offen offset:1024 nt lds\n"
    "s_add_u32 m0, s31, 81920\n"
    "s_add_u32 s29, s28, 81920\n"
    "buffer_load_dwordx4 v8, s[16:19], s29 offen nt lds\n"
    "s_add_u32 m0, s31, 82944\n"
    "s_nop 0\n"
    "buffer_load_dwordx4 v9, s[16:19], s29 offen nt lds\n"
    "s_waitcnt vmcnt(8)\n"
    "s_barrier\n"
    "s_add_u32 m0, s31, 57344\n"
    "s_add_u32 s29, s28, 122880\n"
    "buffer_load_dwordx4 v12, s[12:15], s29 offen nt lds\n"
    "buffer_load_dwordx4 v13, s[12:15], s29 offen offset:1024 nt lds\n"
    "s_add_u32 m0, s31, 90112\n"
    "s_add_u32 s29, s28, 90112\n"
    "buffer_load_dwordx4 v8, s[16:19], s29 offen nt lds\n"
    "s_add_u32 m0, s31, 91136\n"
    "s_nop 0\n"
    "buffer_load_dwordx4 v9, s[16:19], s29 offen nt lds\n"
    "s_waitcnt vmcnt(8)\n"
    "s_barrier\n"
    "s_add_u32 m0, s31, 98304\n"
    "s_add_u32 s29, s28, 98304\n"
    "buffer_load_dwordx4 v8, s[16:19], s29 offen nt lds\n"
    "s_add_u32 m0, s31, 99328\n"
    "s_nop 0\n"
    "buffer_load_dwordx4 v9, s[16:19], s29 offen nt lds\n"
    "s_add_u32 m0, s31, 106496\n"
    "s_add_u32 s29, s28, 106496\n"
    "buffer_load_dwordx4 v8, s[16:19], s29 offen nt lds\n"
    "s_add_u32 m0, s31, 107520\n"
    "s_nop 0\n"
    "buffer_load_dwordx4 v9, s[16:19], s29 offen nt lds\n"
    "s_waitcnt vmcnt(8)\n"
    "s_barrier\n"
    "s_add_u32 m0, s31, 114688\n"
    "s_add_u32 s29, s28, 114688\n"
    "buffer_load_dwordx4 v8, s[16:19], s29 offen nt lds\n"
    "s_add_u32 m0, s31, 115712\n"
    "s_nop 0\n"
    "buffer_load_dwordx4 v9, s[16:19], s29 offen nt lds\n"
    "s_add_u32 m0, s31, 122880\n"
    "s_add_u32 s29, s28, 122880\n"
    "buffer_load_dwordx4 v8, s[16:19], s29 offen nt lds\n"
    "s_add_u32 m0, s31, 123904\n"
    "s_nop 0\n"
    "buffer_load_dwordx4 v9, s[16:19], s29 offen nt lds\n"
    "s_waitcnt vmcnt(6)\n"
    "s_barrier\n"
    "s_waitcnt vmcnt(0)\n"
    "s_barrier\n"
    "s_endpgm\n"
    "2:\n"
    "s_mov_b32 s38, 0xbe0293ee\n"
    "s_mov_b32 s22, 0x800000\n"
    "s_mov_b32 s23, 0x20000\n"
    "s_lshl_b32 s31, s25, 13\n"
    "s_or_b32 s27, s27, s31\n"
    "s_add_u32 s30, s27, 0x1000\n"
    "s_or_b32 s31, s31, 0x20000\n"
    "v_and_b32_e32 v8, 15, v0\n"
    "v_lshrrev_b32_e32 v9, 4, v0\n"
    "v_xor_b32_e32 v6, v8, v9\n"
    "v_lshlrev_b32_e32 v6, 4, v6\n"
    "v_lshl_or_b32 v236, v8, 8, v6\n"
    "v_xor_b32_e32 v237, 64, v236\n"
    "v_xor_b32_e32 v238, 128, v236\n"
    "v_xor_b32_e32 v239, 192, v236\n"
    "v_add_u32_e32 v96, s31, v236\n"
    "v_add_u32_e32 v97, s31, v237\n"
    "v_add_u32_e32 v98, s31, v238\n"
    "v_add_u32_e32 v99, s31, v239\n"
    "v_lshlrev_b32_e32 v241, 4, v0\n"
    "v_and_b32_e32 v241, 0x1c0, v241\n"
    "v_lshrrev_b32_e32 v6, 5, v0\n"
    "v_lshl_or_b32 v241, v6, 11, v241\n"
    "v_lshlrev_b32_e32 v6, 1, v0\n"
    "v_and_b32_e32 v6, 32, v6\n"
    "v_or_b32_e32 v241, v241, v6\n"
    "v_and_b32_e32 v6, 3, v0\n"
    "v_lshl_or_b32 v241, v6, 3, v241\n"
    "v_or_b32_e32 v241, 0x10000, v241\n"
    "v_xor_b32_e32 v242, 32, v241\n"
    "v_and_b32_e32 v6, 7, v0\n"
    "v_lshrrev_b32_e32 v1, 5, v0\n"
    "v_xor_b32_e32 v1, v1, v6\n"
    "v_lshlrev_b32_e32 v1, 4, v1\n"
    "v_lshl_or_b32 v243, v8, 8, v1\n"
    "v_and_b32_e32 v1, 1, v9\n"
    "v_lshl_or_b32 v243, v1, 3, v243\n"
    "v_or_b32_e32 v243, s31, v243\n"
    "v_lshlrev_b32_e32 v6, 4, v6\n"
    "v_lshrrev_b32_e32 v1, 3, v0\n"
    "v_lshl_or_b32 v245, v1, 8, v6\n"
    "v_lshlrev_b32_e32 v1, 4, v1\n"
    "v_xor_b32_e32 v244, v245, v1\n"
    "v_or_b32_e32 v244, s31, v244\n"
    "v_and_b32_e32 v6, 3, v0\n"
    "v_add_u32_e32 v6, -1, v6\n"
    "v_ashrrev_i32_e32 v6, 31, v6\n"
    "v_and_b32_e32 v232, 0x3f803f80, v6\n"
    "v_and_b32_e32 v233, 0x3f803f80, v6\n"
    "v_and_b32_e32 v234, 0x3f803f80, v6\n"
    "v_and_b32_e32 v235, 0x3f803f80, v6\n"
    "s_waitcnt vmcnt(3)\n"
    "v_lshlrev_b32_e32 v48, 16, v32\n"
    "v_and_b32_e32 v49, 0xffff0000, v32\n"
    "v_lshlrev_b32_e32 v50, 16, v33\n"
    "v_and_b32_e32 v51, 0xffff0000, v33\n"
    "v_lshlrev_b32_e32 v52, 16, v34\n"
    "v_and_b32_e32 v53, 0xffff0000, v34\n"
    "v_lshlrev_b32_e32 v54, 16, v35\n"
    "v_and_b32_e32 v55, 0xffff0000, v35\n"
    "v_pk_mul_f32 v[48:49], v[48:49], s[34:35]\n"
    "v_pk_mul_f32 v[50:51], v[50:51], s[34:35]\n"
    "v_pk_mul_f32 v[52:53], v[52:53], s[34:35]\n"
    "v_pk_mul_f32 v[54:55], v[54:55], s[34:35]\n"
    "v_cvt_pk_bf16_f32 v32, v48, v49\n"
    "v_cvt_pk_bf16_f32 v33, v50, v51\n"
    "v_cvt_pk_bf16_f32 v34, v52, v53\n"
    "v_cvt_pk_bf16_f32 v35, v54, v55\n"
    "ds_write_b128 v7, v[32:35] offset:0\n"
    "s_waitcnt vmcnt(2)\n"
    "v_lshlrev_b32_e32 v56, 16, v36\n"
    "v_and_b32_e32 v57, 0xffff0000, v36\n"
    "v_lshlrev_b32_e32 v58, 16, v37\n"
    "v_and_b32_e32 v59, 0xffff0000, v37\n"
    "v_lshlrev_b32_e32 v60, 16, v38\n"
    "v_and_b32_e32 v61, 0xffff0000, v38\n"
    "v_lshlrev_b32_e32 v62, 16, v39\n"
    "v_and_b32_e32 v63, 0xffff0000, v39\n"
    "v_pk_mul_f32 v[56:57], v[56:57], s[34:35]\n"
    "v_pk_mul_f32 v[58:59], v[58:59], s[34:35]\n"
    "v_pk_mul_f32 v[60:61], v[60:61], s[34:35]\n"
    "v_pk_mul_f32 v[62:63], v[62:63], s[34:35]\n"
    "v_cvt_pk_bf16_f32 v36, v56, v57\n"
    "v_cvt_pk_bf16_f32 v37, v58, v59\n"
    "v_cvt_pk_bf16_f32 v38, v60, v61\n"
    "v_cvt_pk_bf16_f32 v39, v62, v63\n"
    "ds_write_b128 v7, v[36:39] offset:1024\n"
    "s_waitcnt vmcnt(1)\n"
    "v_lshlrev_b32_e32 v48, 16, v40\n"
    "v_and_b32_e32 v49, 0xffff0000, v40\n"
    "v_lshlrev_b32_e32 v50, 16, v41\n"
    "v_and_b32_e32 v51, 0xffff0000, v41\n"
    "v_lshlrev_b32_e32 v52, 16, v42\n"
    "v_and_b32_e32 v53, 0xffff0000, v42\n"
    "v_lshlrev_b32_e32 v54, 16, v43\n"
    "v_and_b32_e32 v55, 0xffff0000, v43\n"
    "v_pk_mul_f32 v[48:49], v[48:49], s[34:35]\n"
    "v_pk_mul_f32 v[50:51], v[50:51], s[34:35]\n"
    "v_pk_mul_f32 v[52:53], v[52:53], s[34:35]\n"
    "v_pk_mul_f32 v[54:55], v[54:55], s[34:35]\n"
    "v_cvt_pk_bf16_f32 v40, v48, v49\n"
    "v_cvt_pk_bf16_f32 v41, v50, v51\n"
    "v_cvt_pk_bf16_f32 v42, v52, v53\n"
    "v_cvt_pk_bf16_f32 v43, v54, v55\n"
    "ds_write_b128 v7, v[40:43] offset:2048\n"
    "s_waitcnt vmcnt(0)\n"
    "v_lshlrev_b32_e32 v56, 16, v44\n"
    "v_and_b32_e32 v57, 0xffff0000, v44\n"
    "v_lshlrev_b32_e32 v58, 16, v45\n"
    "v_and_b32_e32 v59, 0xffff0000, v45\n"
    "v_lshlrev_b32_e32 v60, 16, v46\n"
    "v_and_b32_e32 v61, 0xffff0000, v46\n"
    "v_lshlrev_b32_e32 v62, 16, v47\n"
    "v_and_b32_e32 v63, 0xffff0000, v47\n"
    "v_pk_mul_f32 v[56:57], v[56:57], s[34:35]\n"
    "v_pk_mul_f32 v[58:59], v[58:59], s[34:35]\n"
    "v_pk_mul_f32 v[60:61], v[60:61], s[34:35]\n"
    "v_pk_mul_f32 v[62:63], v[62:63], s[34:35]\n"
    "v_cvt_pk_bf16_f32 v44, v56, v57\n"
    "v_cvt_pk_bf16_f32 v45, v58, v59\n"
    "v_cvt_pk_bf16_f32 v46, v60, v61\n"
    "v_cvt_pk_bf16_f32 v47, v62, v63\n"
    "ds_write_b128 v7, v[44:47] offset:3072\n"
    "s_waitcnt lgkmcnt(0)\n"
    "s_barrier\n"
    "ds_read_b128 v[176:179], v236 offset:0\n"
    "ds_read_b128 v[144:147], v96 offset:0\n"
    "ds_read_b128 v[148:151], v96 offset:4096\n"
    "ds_read_b128 v[180:183], v236 offset:4096\n"
    "ds_read_b128 v[184:187], v237 offset:0\n"
    "ds_read_b128 v[152:155], v97 offset:0\n"
    "ds_read_b128 v[156:159], v97 offset:4096\n"
    "ds_read_b128 v[188:191], v237 offset:4096\n"
    "ds_read_b128 v[160:163], v98 offset:0\n"
    "ds_read_b128 v[164:167], v98 offset:4096\n"
    "ds_read_b128 v[168:171], v99 offset:0\n"
    "ds_read_b128 v[172:175], v99 offset:4096\n"
    "; ---- gap 0: QK 0\n"
    "s_waitcnt lgkmcnt(10)\n"
    "v_mfma_f32_16x16x32_bf16 v[0:3], v[176:179], v[144:147], 0\n"
    "s_waitcnt lgkmcnt(9)\n"
    "v_mfma_f32_16x16x32_bf16 v[8:11], v[176:179], v[148:151], 0\n"
    "ds_read_b128 v[192:195], v238 offset:0\n"
    "ds_read_b128 v[196:199], v238 offset:4096\n"
    "s_waitcnt lgkmcnt(10)\n"
    "v_mfma_f32_16x16x32_bf16 v[4:7], v[180:183], v[144:147], 0\n"
    "v_mfma_f32_16x16x32_bf16 v[12:15], v[180:183], v[148:151], 0\n"
    "ds_read_b128 v[200:203], v239 offset:0\n"
    "s_waitcnt lgkmcnt(9)\n"
    "v_mfma_f32_16x16x32_bf16 v[0:3], v[184:187], v[152:155], v[0:3]\n"
    "s_waitcnt lgkmcnt(8)\n"
    "v_mfma_f32_16x16x32_bf16 v[8:11], v[184:187], v[156:159], v[8:11]\n"
    "ds_read_b128 v[204:207], v239 offset:4096\n"
    "s_waitcnt lgkmcnt(8)\n"
    "v_mfma_f32_16x16x32_bf16 v[4:7], v[188:191], v[152:155], v[4:7]\n"
    "v_mfma_f32_16x16x32_bf16 v[12:15], v[188:191], v[156:159], v[12:15]\n"
    "s_barrier\n"
    "ds_read_b128 v[208:211], v236 offset:8192\n"
    "s_waitcnt lgkmcnt(3)\n"
    "v_mfma_f32_16x16x32_bf16 v[0:3], v[192:195], v[160:163], v[0:3]\n"
    "v_mfma_f32_16x16x32_bf16 v[8:11], v[192:195], v[164:167], v[8:11]\n"
    "ds_read_b128 v[212:215], v236 offset:12288\n"
    "v_mfma_f32_16x16x32_bf16 v[4:7], v[196:199], v[160:163], v[4:7]\n"
    "v_mfma_f32_16x16x32_bf16 v[12:15], v[196:199], v[164:167], v[12:15]\n"
    "ds_read_b128 v[176:179], v237 offset:8192\n"
    "s_waitcnt lgkmcnt(3)\n"
    "v_mfma_f32_16x16x32_bf16 v[0:3], v[200:203], v[168:171], v[0:3]\n"
    "v_mfma_f32_16x16x32_bf16 v[8:11], v[200:203], v[172:175], v[8:11]\n"
    "ds_read_b128 v[180:183], v237 offset:12288\n"
    "v_mfma_f32_16x16x32_bf16 v[4:7], v[204:207], v[168:171], v[4:7]\n"
    "v_mfma_f32_16x16x32_bf16 v[12:15], v[204:207], v[172:175], v[12:15]\n"
    "ds_read_b128 v[184:187], v238 offset:8192\n"
    "s_waitcnt lgkmcnt(3)\n"
    "; ---- gap 8: QK 1\n"
    "v_mfma_f32_16x16x32_bf16 v[16:19], v[208:211], v[144:147], 0\n"
    "s_nop 0\n"
    "v_max3_f32 v136, v0, v1, v2\n"
    "v_max3_f32 v138, v8, v9, v10\n"
    "s_nop 0\n"
    "v_max3_f32 v137, v3, v4, v5\n"
    "v_max3_f32 v139, v11, v12, v13\n"
    "v_mfma_f32_16x16x32_bf16 v[24:27], v[208:211], v[148:151], 0\n"
    "ds_read_b128 v[188:191], v238 offset:12288\n"
    "v_max3_f32 v136, v136, v6, v7\n"
    "v_max3_f32 v138, v138, v14, v15\n"
    "v_max_f32_e32 v136, v136, v137\n"
    "v_mfma_f32_16x16x32_bf16 v[20:23], v[212:215], v[144:147], 0\n"
    "v_max_f32_e32 v138, v138, v139\n"
    "v_mov_b32_e32 v137, v136\n"
    "v_mov_b32_e32 v139, v138\n"
    "s_nop 0\n"
    "v_permlane32_swap_b32_e32 v137, v136\n"
    "v_permlane32_swap_b32_e32 v139, v138\n"
    "v_max_f32_e32 v136, v136, v137\n"
    "v_max_f32_e32 v138, v138, v139\n"
    "v_mfma_f32_16x16x32_bf16 v[28:31], v[212:215], v[148:151], 0\n"
    "ds_read_b128 v[192:195], v239 offset:8192\n"
    "s_waitcnt lgkmcnt(3)\n"
    "v_mfma_f32_16x16x32_bf16 v[16:19], v[176:179], v[152:155], v[16:19]\n"
    "v_mov_b32_e32 v137, v136\n"
    "v_mov_b32_e32 v139, v138\n"
    "s_nop 0\n"
    "v_permlane16_swap_b32_e32 v137, v136\n"
    "v_permlane16_swap_b32_e32 v139, v138\n"
    "v_max_f32_e32 v136, v136, v137\n"
    "v_max_f32_e32 v138, v138, v139\n"
    "v_mfma_f32_16x16x32_bf16 v[24:27], v[176:179], v[156:159], v[24:27]\n"
    "ds_read_b128 v[196:199], v239 offset:12288\n"
    "v_mfma_f32_16x16x32_bf16 v[20:23], v[180:183], v[152:155], v[20:23]\n"
    "v_sub_f32_e32 v216, 0, v136\n"
    "v_sub_f32_e32 v220, 0, v138\n"
    "v_mov_b32_e32 v217, v216\n"
    "v_mfma_f32_16x16x32_bf16 v[28:31], v[180:183], v[156:159], v[28:31]\n"
    "s_barrier\n"
    "ds_read_b128 v[200:203], v236 offset:16384\n"
    "s_waitcnt lgkmcnt(3)\n"
    "v_mov_b32_e32 v221, v220\n"
    "v_mfma_f32_16x16x32_bf16 v[16:19], v[184:187], v[160:163], v[16:19]\n"
    "v_mov_b32_e32 v218, v216\n"
    "v_mov_b32_e32 v222, v220\n"
    "v_mov_b32_e32 v219, v216\n"
    "v_mov_b32_e32 v223, v220\n"
    "v_mfma_f32_16x16x32_bf16 v[24:27], v[184:187], v[164:167], v[24:27]\n"
    "ds_read_b128 v[204:207], v236 offset:20480\n"
    "v_add_f32_e32 v0, v0, v216\n"
    "v_add_f32_e32 v1, v1, v216\n"
    "v_add_f32_e32 v2, v2, v216\n"
    "v_add_f32_e32 v3, v3, v216\n"
    "v_mfma_f32_16x16x32_bf16 v[20:23], v[188:191], v[160:163], v[20:23]\n"
    "v_exp_f32_e32 v0, v0\n"
    "v_exp_f32_e32 v1, v1\n"
    "v_mfma_f32_16x16x32_bf16 v[28:31], v[188:191], v[164:167], v[28:31]\n"
    "ds_read_b128 v[208:211], v237 offset:16384\n"
    "s_waitcnt lgkmcnt(3)\n"
    "v_add_f32_e32 v4, v4, v216\n"
    "v_cvt_pk_bf16_f32 v96, v0, v1\n"
    "v_add_f32_e32 v5, v5, v216\n"
    "v_mfma_f32_16x16x32_bf16 v[16:19], v[192:195], v[168:171], v[16:19]\n"
    "v_exp_f32_e32 v2, v2\n"
    "v_exp_f32_e32 v3, v3\n"
    "v_mfma_f32_16x16x32_bf16 v[24:27], v[192:195], v[172:175], v[24:27]\n"
    "ds_read_b128 v[212:215], v237 offset:20480\n"
    "v_add_f32_e32 v6, v6, v216\n"
    "v_cvt_pk_bf16_f32 v97, v2, v3\n"
    "v_add_f32_e32 v7, v7, v216\n"
    "v_mfma_f32_16x16x32_bf16 v[20:23], v[196:199], v[168:171], v[20:23]\n"
    "v_exp_f32_e32 v4, v4\n"
    "v_exp_f32_e32 v5, v5\n"
    "v_mfma_f32_16x16x32_bf16 v[28:31], v[196:199], v[172:175], v[28:31]\n"
    "ds_read_b128 v[176:179], v238 offset:16384\n"
    "s_waitcnt lgkmcnt(3)\n"
    "v_add_f32_e32 v8, v8, v220\n"
    "v_cvt_pk_bf16_f32 v98, v4, v5\n"
    "v_add_f32_e32 v9, v9, v220\n"
    "; ---- gap 16: QK 2\n"
    "v_mfma_f32_16x16x32_bf16 v[32:35], v[200:203], v[144:147], v[216:219]\n"
    "v_exp_f32_e32 v6, v6\n"
    "v_exp_f32_e32 v7, v7\n"
    "v_mfma_f32_16x16x32_bf16 v[40:43], v[200:203], v[148:151], v[220:223]\n"
    "ds_read_b128 v[180:183], v238 offset:20480\n"
    "v_add_f32_e32 v10, v10, v220\n"
    "v_cvt_pk_bf16_f32 v99, v6, v7\n"
    "v_add_f32_e32 v11, v11, v220\n"
    "v_add_f32_e32 v12, v12, v220\n"
    "v_mfma_f32_16x16x32_bf16 v[36:39], v[204:207], v[144:147], v[216:219]\n"
    "v_exp_f32_e32 v8, v8\n"
    "v_exp_f32_e32 v9, v9\n"
    "v_mfma_f32_16x16x32_bf16 v[44:47], v[204:207], v[148:151], v[220:223]\n"
    "ds_read_b128 v[184:187], v239 offset:16384\n"
    "s_waitcnt lgkmcnt(3)\n"
    "v_cvt_pk_bf16_f32 v100, v8, v9\n"
    "v_add_f32_e32 v13, v13, v220\n"
    "v_add_f32_e32 v14, v14, v220\n"
    "v_mfma_f32_16x16x32_bf16 v[32:35], v[208:211], v[152:155], v[32:35]\n"
    "v_exp_f32_e32 v10, v10\n"
    "v_exp_f32_e32 v11, v11\n"
    "v_mfma_f32_16x16x32_bf16 v[40:43], v[208:211], v[156:159], v[40:43]\n"
    "ds_read_b128 v[188:191], v239 offset:20480\n"
    "v_cvt_pk_bf16_f32 v101, v10, v11\n"
    "v_add_f32_e32 v15, v15, v220\n"
    "v_add_f32_e32 v16, v16, v216\n"
    "v_add_f32_e32 v17, v17, v216\n"
    "v_mfma_f32_16x16x32_bf16 v[36:39], v[212:215], v[152:155], v[36:39]\n"
    "v_exp_f32_e32 v12, v12\n"
    "v_exp_f32_e32 v13, v13\n"
    "v_mfma_f32_16x16x32_bf16 v[44:47], v[212:215], v[156:159], v[44:47]\n"
    "s_barrier\n"
    "ds_read_b128 v[192:195], v236 offset:24576\n"
    "s_waitcnt lgkmcnt(3)\n"
    "v_cvt_pk_bf16_f32 v102, v12, v13\n"
    "v_mfma_f32_16x16x32_bf16 v[32:35], v[176:179], v[160:163], v[32:35]\n"
    "v_exp_f32_e32 v14, v14\n"
    "v_exp_f32_e32 v15, v15\n"
    "v_mfma_f32_16x16x32_bf16 v[40:43], v[176:179], v[164:167], v[40:43]\n"
    "ds_read_b128 v[196:199], v236 offset:28672\n"
    "v_add_f32_e32 v18, v18, v216\n"
    "v_cvt_pk_bf16_f32 v103, v14, v15\n"
    "v_add_f32_e32 v19, v19, v216\n"
    "v_add_f32_e32 v20, v20, v216\n"
    "v_mfma_f32_16x16x32_bf16 v[36:39], v[180:183], v[160:163], v[36:39]\n"
    "v_exp_f32_e32 v16, v16\n"
    "v_exp_f32_e32 v17, v17\n"
    "v_mfma_f32_16x16x32_bf16 v[44:47], v[180:183], v[164:167], v[44:47]\n"
    "ds_read_b128 v[200:203], v237 offset:24576\n"
    "s_waitcnt lgkmcnt(3)\n"
    "v_cvt_pk_bf16_f32 v104, v16, v17\n"
    "v_add_f32_e32 v21, v21, v216\n"
    "v_add_f32_e32 v22, v22, v216\n"
    "v_mfma_f32_16x16x32_bf16 v[32:35], v[184:187], v[168:171], v[32:35]\n"
    "v_exp_f32_e32 v18, v18\n"
    "v_exp_f32_e32 v19, v19\n"
    "v_mfma_f32_16x16x32_bf16 v[40:43], v[184:187], v[172:175], v[40:43]\n"
    "ds_read_b128 v[204:207], v237 offset:28672\n"
    "v_cvt_pk_bf16_f32 v105, v18, v19\n"
    "v_add_f32_e32 v23, v23, v216\n"
    "v_add_f32_e32 v24, v24, v220\n"
    "v_add_f32_e32 v25, v25, v220\n"
    "v_mfma_f32_16x16x32_bf16 v[36:39], v[188:191], v[168:171], v[36:39]\n"
    "v_exp_f32_e32 v20, v20\n"
    "v_exp_f32_e32 v21, v21\n"
    "v_mfma_f32_16x16x32_bf16 v[44:47], v[188:191], v[172:175], v[44:47]\n"
    "ds_read_b128 v[208:211], v238 offset:24576\n"
    "s_waitcnt lgkmcnt(3)\n"
    "v_cvt_pk_bf16_f32 v106, v20, v21\n"
    "v_add_f32_e32 v26, v26, v220\n"
    "v_add_f32_e32 v27, v27, v220\n"
    "; ---- gap 24: QK 3\n"
    "v_mfma_f32_16x16x32_bf16 v[48:51], v[192:195], v[144:147], v[216:219]\n"
    "v_exp_f32_e32 v22, v22\n"
    "v_exp_f32_e32 v23, v23\n"
    "v_mfma_f32_16x16x32_bf16 v[56:59], v[192:195], v[148:151], v[220:223]\n"
    "ds_read_b128 v[212:215], v238 offset:28672\n"
    "v_cvt_pk_bf16_f32 v107, v22, v23\n"
    "v_add_f32_e32 v28, v28, v220\n"
    "v_add_f32_e32 v29, v29, v220\n"
    "v_mfma_f32_16x16x32_bf16 v[52:55], v[196:199], v[144:147], v[216:219]\n"
    "v_exp_f32_e32 v24, v24\n"
    "v_exp_f32_e32 v25, v25\n"
    "v_mfma_f32_16x16x32_bf16 v[60:63], v[196:199], v[148:151], v[220:223]\n"
    "ds_read_b128 v[176:179], v239 offset:24576\n"
    "s_waitcnt lgkmcnt(3)\n"
    "v_cvt_pk_bf16_f32 v108, v24, v25\n"
    "v_add_f32_e32 v30, v30, v220\n"
    "v_add_f32_e32 v31, v31, v220\n"
    "v_mfma_f32_16x16x32_bf16 v[48:51], v[200:203], v[152:155], v[48:51]\n"
    "v_exp_f32_e32 v26, v26\n"
    "v_exp_f32_e32 v27, v27\n"
    "v_mfma_f32_16x16x32_bf16 v[56:59], v[200:203], v[156:159], v[56:59]\n"
    "ds_read_b128 v[180:183], v239 offset:28672\n"
    "v_cvt_pk_bf16_f32 v109, v26, v27\n"
    "v_exp_f32_e32 v28, v28\n"
    "v_mfma_f32_16x16x32_bf16 v[52:55], v[204:207], v[152:155], v[52:55]\n"
    "v_exp_f32_e32 v29, v29\n"
    "v_exp_f32_e32 v30, v30\n"
    "v_cvt_pk_bf16_f32 v110, v28, v29\n"
    "v_mfma_f32_16x16x32_bf16 v[60:63], v[204:207], v[156:159], v[60:63]\n"
    "s_barrier\n"
    "ds_read_b128 v[184:187], v236 offset:32768\n"
    "s_waitcnt lgkmcnt(3)\n"
    "v_exp_f32_e32 v31, v31\n"
    "v_mfma_f32_16x16x32_bf16 v[48:51], v[208:211], v[160:163], v[48:51]\n"
    "v_exp_f32_e32 v32, v32\n"
    "v_exp_f32_e32 v33, v33\n"
    "v_mfma_f32_16x16x32_bf16 v[56:59], v[208:211], v[164:167], v[56:59]\n"
    "ds_read_b128 v[188:191], v236 offset:36864\n"
    "v_cvt_pk_bf16_f32 v111, v30, v31\n"
    "v_cvt_pk_bf16_f32 v112, v32, v33\n"
    "v_exp_f32_e32 v34, v34\n"
    "v_mfma_f32_16x16x32_bf16 v[52:55], v[212:215], v[160:163], v[52:55]\n"
    "v_exp_f32_e32 v35, v35\n"
    "v_exp_f32_e32 v36, v36\n"
    "v_mfma_f32_16x16x32_bf16 v[60:63], v[212:215], v[164:167], v[60:63]\n"
    "ds_read_b128 v[192:195], v237 offset:32768\n"
    "s_waitcnt lgkmcnt(3)\n"
    "v_cvt_pk_bf16_f32 v113, v34, v35\n"
    "v_exp_f32_e32 v37, v37\n"
    "v_mfma_f32_16x16x32_bf16 v[48:51], v[176:179], v[168:171], v[48:51]\n"
    "v_exp_f32_e32 v38, v38\n"
    "v_exp_f32_e32 v39, v39\n"
    "v_mfma_f32_16x16x32_bf16 v[56:59], v[176:179], v[172:175], v[56:59]\n"
    "ds_read_b128 v[196:199], v237 offset:36864\n"
    "v_cvt_pk_bf16_f32 v114, v36, v37\n"
    "v_cvt_pk_bf16_f32 v115, v38, v39\n"
    "v_mfma_f32_16x16x32_bf16 v[52:55], v[180:183], v[168:171], v[52:55]\n"
    "v_exp_f32_e32 v40, v40\n"
    "v_exp_f32_e32 v41, v41\n"
    "v_mfma_f32_16x16x32_bf16 v[60:63], v[180:183], v[172:175], v[60:63]\n"
    "ds_read_b128 v[200:203], v238 offset:32768\n"
    "s_waitcnt lgkmcnt(3)\n"
    "v_cvt_pk_bf16_f32 v116, v40, v41\n"
    "v_exp_f32_e32 v42, v42\n"
    "; ---- gap 32: QK 4\n"
    "v_mfma_f32_16x16x32_bf16 v[64:67], v[184:187], v[144:147], v[216:219]\n"
    "v_exp_f32_e32 v43, v43\n"
    "v_exp_f32_e32 v44, v44\n"
    "v_cvt_pk_bf16_f32 v117, v42, v43\n"
    "v_mfma_f32_16x16x32_bf16 v[72:75], v[184:187], v[148:151], v[220:223]\n"
    "ds_read_b128 v[204:207], v238 offset:36864\n"
    "v_exp_f32_e32 v45, v45\n"
    "v_exp_f32_e32 v46, v46\n"
    "v_mfma_f32_16x16x32_bf16 v[68:71], v[188:191], v[144:147], v[216:219]\n"
    "v_exp_f32_e32 v47, v47\n"
    "v_exp_f32_e32 v48, v48\n"
    "v_mfma_f32_16x16x32_bf16 v[76:79], v[188:191], v[148:151], v[220:223]\n"
    "ds_read_b128 v[208:211], v239 offset:32768\n"
    "s_waitcnt lgkmcnt(3)\n"
    "v_cvt_pk_bf16_f32 v118, v44, v45\n"
    "v_cvt_pk_bf16_f32 v119, v46, v47\n"
    "v_mfma_f32_16x16x32_bf16 v[64:67], v[192:195], v[152:155], v[64:67]\n"
    "v_exp_f32_e32 v49, v49\n"
    "v_exp_f32_e32 v50, v50\n"
    "v_mfma_f32_16x16x32_bf16 v[72:75], v[192:195], v[156:159], v[72:75]\n"
    "ds_read_b128 v[212:215], v239 offset:36864\n"
    "s_waitcnt lgkmcnt(3)\n"
    "v_cvt_pk_bf16_f32 v120, v48, v49\n"
    "v_exp_f32_e32 v51, v51\n"
    "v_mfma_f32_16x16x32_bf16 v[68:71], v[196:199], v[152:155], v[68:71]\n"
    "v_exp_f32_e32 v52, v52\n"
    "v_exp_f32_e32 v53, v53\n"
    "v_mfma_f32_16x16x32_bf16 v[76:79], v[196:199], v[156:159], v[76:79]\n"
    "ds_read_b64_tr_b16 v[176:177], v241 offset:0\n"
    "ds_read_b64_tr_b16 v[178:179], v241 offset:4096\n"
    "v_cvt_pk_bf16_f32 v121, v50, v51\n"
    "v_cvt_pk_bf16_f32 v122, v52, v53\n"
    "v_mfma_f32_16x16x32_bf16 v[64:67], v[200:203], v[160:163], v[64:67]\n"
    "v_exp_f32_e32 v54, v54\n"
    "v_exp_f32_e32 v55, v55\n"
    "v_mfma_f32_16x16x32_bf16 v[72:75], v[200:203], v[164:167], v[72:75]\n"
    "ds_read_b64_tr_b16 v[180:181], v242 offset:0\n"
    "ds_read_b64_tr_b16 v[182:183], v242 offset:4096\n"
    "s_waitcnt lgkmcnt(5)\n"
    "v_cvt_pk_bf16_f32 v123, v54, v55\n"
    "v_exp_f32_e32 v56, v56\n"
    "v_mfma_f32_16x16x32_bf16 v[68:71], v[204:207], v[160:163], v[68:71]\n"
    "v_exp_f32_e32 v57, v57\n"
    "v_exp_f32_e32 v58, v58\n"
    "v_mfma_f32_16x16x32_bf16 v[76:79], v[204:207], v[164:167], v[76:79]\n"
    "ds_read_b64_tr_b16 v[184:185], v241 offset:512\n"
    "ds_read_b64_tr_b16 v[186:187], v241 offset:4608\n"
    "v_cvt_pk_bf16_f32 v124, v56, v57\n"
    "v_exp_f32_e32 v59, v59\n"
    "v_mfma_f32_16x16x32_bf16 v[64:67], v[208:211], v[168:171], v[64:67]\n"
    "v_exp_f32_e32 v60, v60\n"
    "v_exp_f32_e32 v61, v61\n"
    "v_mfma_f32_16x16x32_bf16 v[72:75], v[208:211], v[172:175], v[72:75]\n"
    "ds_read_b64_tr_b16 v[188:189], v242 offset:512\n"
    "ds_read_b64_tr_b16 v[190:191], v242 offset:4608\n"
    "s_waitcnt lgkmcnt(6)\n"
    "v_cvt_pk_bf16_f32 v125, v58, v59\n"
    "v_cvt_pk_bf16_f32 v126, v60, v61\n"
    "v_mfma_f32_16x16x32_bf16 v[68:71], v[212:215], v[168:171], v[68:71]\n"
    "v_exp_f32_e32 v62, v62\n"
    "v_exp_f32_e32 v63, v63\n"
    "v_mfma_f32_16x16x32_bf16 v[76:79], v[212:215], v[172:175], v[76:79]\n"
    "ds_read_b64_tr_b16 v[192:193], v241 offset:1024\n"
    "ds_read_b64_tr_b16 v[194:195], v241 offset:5120\n"
    "v_cvt_pk_bf16_f32 v127, v62, v63\n"
    "; ---- gap 40: PV 0\n"
    "v_mfma_f32_16x16x32_bf16 v[0:3], v[176:179], v[96:99], 0\n"
    "v_mfma_f32_16x16x32_bf16 v[4:7], v[176:179], v[100:103], 0\n"
    "ds_read_b64_tr_b16 v[196:197], v242 offset:1024\n"
    "ds_read_b64_tr_b16 v[198:199], v242 offset:5120\n"
    "s_waitcnt lgkmcnt(6)\n"
    "v_mfma_f32_16x16x32_bf16 v[8:11], v[180:183], v[96:99], 0\n"
    "v_exp_f32_e32 v64, v64\n"
    "v_mfma_f32_16x16x32_bf16 v[224:227], v[232:235], v[96:99], 0\n"
    "v_exp_f32_e32 v65, v65\n"
    "v_mfma_f32_16x16x32_bf16 v[12:15], v[180:183], v[100:103], 0\n"
    "ds_read_b64_tr_b16 v[200:201], v241 offset:1536\n"
    "ds_read_b64_tr_b16 v[202:203], v241 offset:5632\n"
    "v_cvt_pk_bf16_f32 v128, v64, v65\n"
    "v_mfma_f32_16x16x32_bf16 v[16:19], v[184:187], v[96:99], 0\n"
    "v_mfma_f32_16x16x32_bf16 v[20:23], v[184:187], v[100:103], 0\n"
    "ds_read_b64_tr_b16 v[204:205], v242 offset:1536\n"
    "ds_read_b64_tr_b16 v[206:207], v242 offset:5632\n"
    "s_waitcnt lgkmcnt(6)\n"
    "v_mfma_f32_16x16x32_bf16 v[24:27], v[188:191], v[96:99], 0\n"
    "v_exp_f32_e32 v66, v66\n"
    "v_mfma_f32_16x16x32_bf16 v[228:231], v[232:235], v[100:103], 0\n"
    "v_exp_f32_e32 v67, v67\n"
    "v_mfma_f32_16x16x32_bf16 v[28:31], v[188:191], v[100:103], 0\n"
    "s_barrier\n"
    "ds_read_b128 v[208:211], v236 offset:40960\n"
    "v_cvt_pk_bf16_f32 v129, v66, v67\n"
    "v_mfma_f32_16x16x32_bf16 v[32:35], v[192:195], v[96:99], 0\n"
    "v_exp_f32_e32 v68, v68\n"
    "v_mfma_f32_16x16x32_bf16 v[36:39], v[192:195], v[100:103], 0\n"
    "ds_read_b128 v[212:215], v236 offset:45056\n"
    "s_waitcnt lgkmcnt(4)\n"
    "v_mfma_f32_16x16x32_bf16 v[40:43], v[196:199], v[96:99], 0\n"
    "v_exp_f32_e32 v69, v69\n"
    "v_mfma_f32_16x16x32_bf16 v[44:47], v[196:199], v[100:103], 0\n"
    "ds_read_b128 v[176:179], v237 offset:40960\n"
    "v_cvt_pk_bf16_f32 v130, v68, v69\n"
    "v_mfma_f32_16x16x32_bf16 v[48:51], v[200:203], v[96:99], 0\n"
    "v_exp_f32_e32 v70, v70\n"
    "v_mfma_f32_16x16x32_bf16 v[52:55], v[200:203], v[100:103], 0\n"
    "ds_read_b128 v[180:183], v237 offset:45056\n"
    "s_waitcnt lgkmcnt(3)\n"
    "v_mfma_f32_16x16x32_bf16 v[56:59], v[204:207], v[96:99], 0\n"
    "v_exp_f32_e32 v71, v71\n"
    "v_mfma_f32_16x16x32_bf16 v[60:63], v[204:207], v[100:103], 0\n"
    "ds_read_b128 v[184:187], v238 offset:40960\n"
    "s_waitcnt lgkmcnt(3)\n"
    "v_cvt_pk_bf16_f32 v131, v70, v71\n"
    "; ---- gap 48: QK 5\n"
    "v_mfma_f32_16x16x32_bf16 v[80:83], v[208:211], v[144:147], v[216:219]\n"
    "v_exp_f32_e32 v72, v72\n"
    "v_mfma_f32_16x16x32_bf16 v[88:91], v[208:211], v[148:151], v[220:223]\n"
    "ds_read_b128 v[188:191], v238 offset:45056\n"
    "ds_read_b128 v[192:195], v239 offset:40960\n"
    "v_mfma_f32_16x16x32_bf16 v[84:87], v[212:215], v[144:147], v[216:219]\n"
    "v_mfma_f32_16x16x32_bf16 v[92:95], v[212:215], v[148:151], v[220:223]\n"
    "ds_read_b128 v[196:199], v239 offset:45056\n"
    "ds_read_b64_tr_b16 v[200:201], v241 offset:8192\n"
    "s_waitcnt lgkmcnt(5)\n"
    "v_mfma_f32_16x16x32_bf16 v[80:83], v[176:179], v[152:155], v[80:83]\n"
    "v_exp_f32_e32 v73, v73\n"
    "v_mfma_f32_16x16x32_bf16 v[88:91], v[176:179], v[156:159], v[88:91]\n"
    "ds_read_b64_tr_b16 v[202:203], v241 offset:12288\n"
    "ds_read_b64_tr_b16 v[204:205], v242 offset:8192\n"
    "v_cvt_pk_bf16_f32 v132, v72, v73\n"
    "v_mfma_f32_16x16x32_bf16 v[84:87], v[180:183], v[152:155], v[84:87]\n"
    "v_mfma_f32_16x16x32_bf16 v[92:95], v[180:183], v[156:159], v[92:95]\n"
    "ds_read_b64_tr_b16 v[206:207], v242 offset:12288\n"
    "ds_read_b64_tr_b16 v[208:209], v241 offset:8704\n"
    "s_waitcnt lgkmcnt(6)\n"
    "v_mfma_f32_16x16x32_bf16 v[80:83], v[184:187], v[160:163], v[80:83]\n"
    "v_exp_f32_e32 v74, v74\n"
    "v_mfma_f32_16x16x32_bf16 v[88:91], v[184:187], v[164:167], v[88:91]\n"
    "ds_read_b64_tr_b16 v[210:211], v241 offset:12800\n"
    "ds_read_b64_tr_b16 v[212:213], v242 offset:8704\n"
    "v_mfma_f32_16x16x32_bf16 v[84:87], v[188:191], v[160:163], v[84:87]\n"
    "v_exp_f32_e32 v75, v75\n"
    "v_mfma_f32_16x16x32_bf16 v[92:95], v[188:191], v[164:167], v[92:95]\n"
    "ds_read_b64_tr_b16 v[214:215], v242 offset:12800\n"
    "ds_read_b64_tr_b16 v[176:177], v241 offset:9216\n"
    "v_cvt_pk_bf16_f32 v133, v74, v75\n"
    "v_mfma_f32_16x16x32_bf16 v[80:83], v[192:195], v[168:171], v[80:83]\n"
    "v_mfma_f32_16x16x32_bf16 v[88:91], v[192:195], v[172:175], v[88:91]\n"
    "ds_read_b64_tr_b16 v[178:179], v241 offset:13312\n"
    "ds_read_b64_tr_b16 v[180:181], v242 offset:9216\n"
    "s_waitcnt lgkmcnt(7)\n"
    "v_mfma_f32_16x16x32_bf16 v[84:87], v[196:199], v[168:171], v[84:87]\n"
    "v_exp_f32_e32 v76, v76\n"
    "v_mfma_f32_16x16x32_bf16 v[92:95], v[196:199], v[172:175], v[92:95]\n"
    "ds_read_b64_tr_b16 v[182:183], v242 offset:13312\n"
    "ds_read_b64_tr_b16 v[184:185], v241 offset:9728\n"
    "; ---- gap 56: PV 1\n"
    "v_mfma_f32_16x16x32_bf16 v[0:3], v[200:203], v[104:107], v[0:3]\n"
    "v_exp_f32_e32 v77, v77\n"
    "v_mfma_f32_16x16x32_bf16 v[4:7], v[200:203], v[108:111], v[4:7]\n"
    "ds_read_b64_tr_b16 v[186:187], v241 offset:13824\n"
    "ds_read_b64_tr_b16 v[188:189], v242 offset:9728\n"
    "v_cvt_pk_bf16_f32 v134, v76, v77\n"
    "v_mfma_f32_16x16x32_bf16 v[8:11], v[204:207], v[104:107], v[8:11]\n"
    "v_exp_f32_e32 v78, v78\n"
    "v_mfma_f32_16x16x32_bf16 v[224:227], v[232:235], v[104:107], v[224:227]\n"
    "v_exp_f32_e32 v79, v79\n"
    "v_mfma_f32_16x16x32_bf16 v[12:15], v[204:207], v[108:111], v[12:15]\n"
    "ds_read_b64_tr_b16 v[190:191], v242 offset:13824\n"
    "s_waitcnt lgkmcnt(6)\n"
    "v_mfma_f32_16x16x32_bf16 v[16:19], v[208:211], v[104:107], v[16:19]\n"
    "v_cvt_pk_bf16_f32 v135, v78, v79\n"
    "v_mfma_f32_16x16x32_bf16 v[20:23], v[208:211], v[108:111], v[20:23]\n"
    "s_barrier\n"
    "ds_read_b128 v[192:195], v236 offset:49152\n"
    "ds_read_b128 v[196:199], v236 offset:53248\n"
    "v_mfma_f32_16x16x32_bf16 v[24:27], v[212:215], v[104:107], v[24:27]\n"
    "v_exp_f32_e32 v80, v80\n"
    "v_mfma_f32_16x16x32_bf16 v[228:231], v[232:235], v[108:111], v[228:231]\n"
    "v_exp_f32_e32 v81, v81\n"
    "v_mfma_f32_16x16x32_bf16 v[28:31], v[212:215], v[108:111], v[28:31]\n"
    "ds_read_b128 v[200:203], v237 offset:49152\n"
    "ds_read_b128 v[204:207], v237 offset:53248\n"
    "v_cvt_pk_bf16_f32 v136, v80, v81\n"
    "v_mfma_f32_16x16x32_bf16 v[32:35], v[176:179], v[104:107], v[32:35]\n"
    "v_mfma_f32_16x16x32_bf16 v[36:39], v[176:179], v[108:111], v[36:39]\n"
    "ds_read_b128 v[208:211], v238 offset:49152\n"
    "ds_read_b128 v[212:215], v238 offset:53248\n"
    "s_waitcnt lgkmcnt(6)\n"
    "v_mfma_f32_16x16x32_bf16 v[40:43], v[180:183], v[104:107], v[40:43]\n"
    "v_exp_f32_e32 v82, v82\n"
    "v_mfma_f32_16x16x32_bf16 v[44:47], v[180:183], v[108:111], v[44:47]\n"
    "ds_read_b128 v[176:179], v239 offset:49152\n"
    "ds_read_b128 v[180:183], v239 offset:53248\n"
    "v_mfma_f32_16x16x32_bf16 v[48:51], v[184:187], v[104:107], v[48:51]\n"
    "v_exp_f32_e32 v83, v83\n"
    "v_mfma_f32_16x16x32_bf16 v[52:55], v[184:187], v[108:111], v[52:55]\n"
    "ds_read_b64_tr_b16 v[184:185], v241 offset:16384\n"
    "ds_read_b64_tr_b16 v[186:187], v241 offset:20480\n"
    "v_cvt_pk_bf16_f32 v137, v82, v83\n"
    "v_mfma_f32_16x16x32_bf16 v[56:59], v[188:191], v[104:107], v[56:59]\n"
    "v_mfma_f32_16x16x32_bf16 v[60:63], v[188:191], v[108:111], v[60:63]\n"
    "ds_read_b64_tr_b16 v[188:189], v242 offset:16384\n"
    "ds_read_b64_tr_b16 v[190:191], v242 offset:20480\n"
    "s_waitcnt lgkmcnt(8)\n"
    "; ---- gap 64: QK 6\n"
    "v_mfma_f32_16x16x32_bf16 v[64:67], v[192:195], v[144:147], v[216:219]\n"
    "v_exp_f32_e32 v84, v84\n"
    "v_mfma_f32_16x16x32_bf16 v[72:75], v[192:195], v[148:151], v[220:223]\n"
    "ds_read_b64_tr_b16 v[192:193], v241 offset:16896\n"
    "ds_read_b64_tr_b16 v[194:195], v241 offset:20992\n"
    "v_mfma_f32_16x16x32_bf16 v[68:71], v[196:199], v[144:147], v[216:219]\n"
    "v_exp_f32_e32 v85, v85\n"
    "v_mfma_f32_16x16x32_bf16 v[76:79], v[196:199], v[148:151], v[220:223]\n"
    "ds_read_b64_tr_b16 v[196:197], v242 offset:16896\n"
    "ds_read_b64_tr_b16 v[198:199], v242 offset:20992\n"
    "v_cvt_pk_bf16_f32 v138, v84, v85\n"
    "v_mfma_f32_16x16x32_bf16 v[64:67], v[200:203], v[152:155], v[64:67]\n"
    "v_exp_f32_e32 v86, v86\n"
    "v_mfma_f32_16x16x32_bf16 v[72:75], v[200:203], v[156:159], v[72:75]\n"
    "ds_read_b64_tr_b16 v[200:201], v241 offset:17408\n"
    "ds_read_b64_tr_b16 v[202:203], v241 offset:21504\n"
    "v_mfma_f32_16x16x32_bf16 v[68:71], v[204:207], v[152:155], v[68:71]\n"
    "v_mfma_f32_16x16x32_bf16 v[76:79], v[204:207], v[156:159], v[76:79]\n"
    "ds_read_b64_tr_b16 v[204:205], v242 offset:17408\n"
    "ds_read_b64_tr_b16 v[206:207], v242 offset:21504\n"
    "s_waitcnt lgkmcnt(12)\n"
    "v_mfma_f32_16x16x32_bf16 v[64:67], v[208:211], v[160:163], v[64:67]\n"
    "v_exp_f32_e32 v87, v87\n"
    "v_mfma_f32_16x16x32_bf16 v[72:75], v[208:211], v[164:167], v[72:75]\n"
    "ds_read_b64_tr_b16 v[208:209], v241 offset:17920\n"
    "ds_read_b64_tr_b16 v[210:211], v241 offset:22016\n"
    "v_cvt_pk_bf16_f32 v139, v86, v87\n"
    "v_mfma_f32_16x16x32_bf16 v[68:71], v[212:215], v[160:163], v[68:71]\n"
    "v_exp_f32_e32 v88, v88\n"
    "v_mfma_f32_16x16x32_bf16 v[76:79], v[212:215], v[164:167], v[76:79]\n"
    "ds_read_b64_tr_b16 v[212:213], v242 offset:17920\n"
    "ds_read_b64_tr_b16 v[214:215], v242 offset:22016\n"
    "v_mfma_f32_16x16x32_bf16 v[64:67], v[176:179], v[168:171], v[64:67]\n"
    "v_exp_f32_e32 v89, v89\n"
    "v_mfma_f32_16x16x32_bf16 v[72:75], v[176:179], v[172:175], v[72:75]\n"
    "s_barrier\n"
    "ds_read_b128 v[176:179], v236 offset:57344\n"
    "v_mfma_f32_16x16x32_bf16 v[68:71], v[180:183], v[168:171], v[68:71]\n"
    "v_exp_f32_e32 v90, v90\n"
    "v_mfma_f32_16x16x32_bf16 v[76:79], v[180:183], v[172:175], v[76:79]\n"
    "ds_read_b128 v[180:183], v236 offset:61440\n"
    "s_waitcnt lgkmcnt(10)\n"
    "v_cvt_pk_bf16_f32 v140, v88, v89\n"
    "; ---- gap 72: PV 2\n"
    "v_mfma_f32_16x16x32_bf16 v[0:3], v[184:187], v[112:115], v[0:3]\n"
    "v_exp_f32_e32 v91, v91\n"
    "v_mfma_f32_16x16x32_bf16 v[4:7], v[184:187], v[116:119], v[4:7]\n"
    "ds_read_b128 v[184:187], v237 offset:57344\n"
    "v_cvt_pk_bf16_f32 v141, v90, v91\n"
    "v_mfma_f32_16x16x32_bf16 v[8:11], v[188:191], v[112:115], v[8:11]\n"
    "v_exp_f32_e32 v92, v92\n"
    "v_mfma_f32_16x16x32_bf16 v[224:227], v[232:235], v[112:115], v[224:227]\n"
    "v_exp_f32_e32 v93, v93\n"
    "v_mfma_f32_16x16x32_bf16 v[12:15], v[188:191], v[116:119], v[12:15]\n"
    "ds_read_b128 v[188:191], v237 offset:61440\n"
    "v_cvt_pk_bf16_f32 v142, v92, v93\n"
    "v_mfma_f32_16x16x32_bf16 v[16:19], v[192:195], v[112:115], v[16:19]\n"
    "v_exp_f32_e32 v94, v94\n"
    "v_mfma_f32_16x16x32_bf16 v[20:23], v[192:195], v[116:119], v[20:23]\n"
    "ds_read_b128 v[192:195], v238 offset:57344\n"
    "v_mfma_f32_16x16x32_bf16 v[24:27], v[196:199], v[112:115], v[24:27]\n"
    "v_exp_f32_e32 v95, v95\n"
    "v_mfma_f32_16x16x32_bf16 v[228:231], v[232:235], v[116:119], v[228:231]\n"
    "v_exp_f32_e32 v64, v64\n"
    "v_mfma_f32_16x16x32_bf16 v[28:31], v[196:199], v[116:119], v[28:31]\n"
    "ds_read_b128 v[196:199], v238 offset:61440\n"
    "s_waitcnt lgkmcnt(6)\n"
    "v_cvt_pk_bf16_f32 v143, v94, v95\n"
    "v_mfma_f32_16x16x32_bf16 v[32:35], v[200:203], v[112:115], v[32:35]\n"
    "v_exp_f32_e32 v65, v65\n"
    "v_mfma_f32_16x16x32_bf16 v[36:39], v[200:203], v[116:119], v[36:39]\n"
    "ds_read_b128 v[200:203], v239 offset:57344\n"
    "v_cvt_pk_bf16_f32 v96, v64, v65\n"
    "v_mfma_f32_16x16x32_bf16 v[40:43], v[204:207], v[112:115], v[40:43]\n"
    "v_exp_f32_e32 v66, v66\n"
    "v_mfma_f32_16x16x32_bf16 v[44:47], v[204:207], v[116:119], v[44:47]\n"
    "ds_read_b128 v[204:207], v239 offset:61440\n"
    "v_mfma_f32_16x16x32_bf16 v[48:51], v[208:211], v[112:115], v[48:51]\n"
    "v_exp_f32_e32 v67, v67\n"
    "v_mfma_f32_16x16x32_bf16 v[52:55], v[208:211], v[116:119], v[52:55]\n"
    "ds_read_b64_tr_b16 v[208:209], v241 offset:24576\n"
    "ds_read_b64_tr_b16 v[210:211], v241 offset:28672\n"
    "v_cvt_pk_bf16_f32 v97, v66, v67\n"
    "v_mfma_f32_16x16x32_bf16 v[56:59], v[212:215], v[112:115], v[56:59]\n"
    "v_exp_f32_e32 v68, v68\n"
    "v_mfma_f32_16x16x32_bf16 v[60:63], v[212:215], v[116:119], v[60:63]\n"
    "ds_read_b64_tr_b16 v[212:213], v242 offset:24576\n"
    "s_waitcnt lgkmcnt(7)\n"
    "; ---- gap 80: QK 7\n"
    "v_mfma_f32_16x16x32_bf16 v[80:83], v[176:179], v[144:147], v[216:219]\n"
    "v_exp_f32_e32 v69, v69\n"
    "v_mfma_f32_16x16x32_bf16 v[88:91], v[176:179], v[148:151], v[220:223]\n"
    "ds_read_b64_tr_b16 v[214:215], v242 offset:28672\n"
    "ds_read_b64_tr_b16 v[176:177], v241 offset:25088\n"
    "v_mfma_f32_16x16x32_bf16 v[84:87], v[180:183], v[144:147], v[216:219]\n"
    "v_exp_f32_e32 v70, v70\n"
    "v_mfma_f32_16x16x32_bf16 v[92:95], v[180:183], v[148:151], v[220:223]\n"
    "ds_read_b64_tr_b16 v[178:179], v241 offset:29184\n"
    "v_cvt_pk_bf16_f32 v98, v68, v69\n"
    "v_mfma_f32_16x16x32_bf16 v[80:83], v[184:187], v[152:155], v[80:83]\n"
    "v_exp_f32_e32 v71, v71\n"
    "v_mfma_f32_16x16x32_bf16 v[88:91], v[184:187], v[156:159], v[88:91]\n"
    "ds_read_b64_tr_b16 v[180:181], v242 offset:25088\n"
    "ds_read_b64_tr_b16 v[182:183], v242 offset:29184\n"
    "v_cvt_pk_bf16_f32 v99, v70, v71\n"
    "v_mfma_f32_16x16x32_bf16 v[84:87], v[188:191], v[152:155], v[84:87]\n"
    "v_exp_f32_e32 v72, v72\n"
    "v_mfma_f32_16x16x32_bf16 v[92:95], v[188:191], v[156:159], v[92:95]\n"
    "ds_read_b64_tr_b16 v[184:185], v241 offset:25600\n"
    "s_waitcnt lgkmcnt(9)\n"
    "v_mfma_f32_16x16x32_bf16 v[80:83], v[192:195], v[160:163], v[80:83]\n"
    "v_exp_f32_e32 v73, v73\n"
    "v_mfma_f32_16x16x32_bf16 v[88:91], v[192:195], v[164:167], v[88:91]\n"
    "ds_read_b64_tr_b16 v[186:187], v241 offset:29696\n"
    "ds_read_b64_tr_b16 v[188:189], v242 offset:25600\n"
    "v_mfma_f32_16x16x32_bf16 v[84:87], v[196:199], v[160:163], v[84:87]\n"
    "v_exp_f32_e32 v74, v74\n"
    "v_mfma_f32_16x16x32_bf16 v[92:95], v[196:199], v[164:167], v[92:95]\n"
    "ds_read_b64_tr_b16 v[190:191], v242 offset:29696\n"
    "v_cvt_pk_bf16_f32 v100, v72, v73\n"
    "v_mfma_f32_16x16x32_bf16 v[80:83], v[200:203], v[168:171], v[80:83]\n"
    "v_exp_f32_e32 v75, v75\n"
    "v_mfma_f32_16x16x32_bf16 v[88:91], v[200:203], v[172:175], v[88:91]\n"
    "ds_read_b64_tr_b16 v[192:193], v241 offset:26112\n"
    "ds_read_b64_tr_b16 v[194:195], v241 offset:30208\n"
    "v_mfma_f32_16x16x32_bf16 v[84:87], v[204:207], v[168:171], v[84:87]\n"
    "v_exp_f32_e32 v76, v76\n"
    "v_mfma_f32_16x16x32_bf16 v[92:95], v[204:207], v[172:175], v[92:95]\n"
    "ds_read_b64_tr_b16 v[196:197], v242 offset:26112\n"
    "s_waitcnt lgkmcnt(7)\n"
    "; ---- gap 88: PV 3\n"
    "v_mfma_f32_16x16x32_bf16 v[0:3], v[208:211], v[120:123], v[0:3]\n"
    "v_exp_f32_e32 v77, v77\n"
    "v_mfma_f32_16x16x32_bf16 v[4:7], v[208:211], v[124:127], v[4:7]\n"
    "ds_read_b64_tr_b16 v[198:199], v242 offset:30208\n"
    "v_cvt_pk_bf16_f32 v101, v74, v75\n"
    "v_mfma_f32_16x16x32_bf16 v[8:11], v[212:215], v[120:123], v[8:11]\n"
    "v_exp_f32_e32 v78, v78\n"
    "v_mfma_f32_16x16x32_bf16 v[224:227], v[232:235], v[120:123], v[224:227]\n"
    "v_exp_f32_e32 v79, v79\n"
    "v_mfma_f32_16x16x32_bf16 v[12:15], v[212:215], v[124:127], v[12:15]\n"
    "s_barrier\n"
    "ds_read_b128 v[200:203], v236 offset:0\n"
    "ds_read_b128 v[204:207], v236 offset:4096\n"
    "v_mfma_f32_16x16x32_bf16 v[16:19], v[176:179], v[120:123], v[16:19]\n"
    "v_exp_f32_e32 v80, v80\n"
    "v_mfma_f32_16x16x32_bf16 v[20:23], v[176:179], v[124:127], v[20:23]\n"
    "ds_read_b128 v[208:211], v237 offset:0\n"
    "s_waitcnt lgkmcnt(5)\n"
    "v_mfma_f32_16x16x32_bf16 v[24:27], v[180:183], v[120:123], v[24:27]\n"
    "v_exp_f32_e32 v81, v81\n"
    "v_mfma_f32_16x16x32_bf16 v[228:231], v[232:235], v[124:127], v[228:231]\n"
    "v_cvt_pk_bf16_f32 v102, v76, v77\n"
    "v_cvt_pk_bf16_f32 v103, v78, v79\n"
    "v_mfma_f32_16x16x32_bf16 v[28:31], v[180:183], v[124:127], v[28:31]\n"
    "ds_read_b128 v[212:215], v237 offset:4096\n"
    "ds_read_b128 v[176:179], v238 offset:0\n"
    "v_mfma_f32_16x16x32_bf16 v[32:35], v[184:187], v[120:123], v[32:35]\n"
    "v_exp_f32_e32 v82, v82\n"
    "v_mfma_f32_16x16x32_bf16 v[36:39], v[184:187], v[124:127], v[36:39]\n"
    "ds_read_b128 v[180:183], v238 offset:4096\n"
    "v_cvt_pk_bf16_f32 v104, v80, v81\n"
    "v_mfma_f32_16x16x32_bf16 v[40:43], v[188:191], v[120:123], v[40:43]\n"
    "v_exp_f32_e32 v83, v83\n"
    "v_mfma_f32_16x16x32_bf16 v[44:47], v[188:191], v[124:127], v[44:47]\n"
    "ds_read_b128 v[184:187], v239 offset:0\n"
    "ds_read_b128 v[188:191], v239 offset:4096\n"
    "v_mfma_f32_16x16x32_bf16 v[48:51], v[192:195], v[120:123], v[48:51]\n"
    "v_exp_f32_e32 v84, v84\n"
    "v_mfma_f32_16x16x32_bf16 v[52:55], v[192:195], v[124:127], v[52:55]\n"
    "ds_read_b64_tr_b16 v[192:193], v241 offset:32768\n"
    "s_waitcnt lgkmcnt(6)\n"
    "v_mfma_f32_16x16x32_bf16 v[56:59], v[196:199], v[120:123], v[56:59]\n"
    "v_exp_f32_e32 v85, v85\n"
    "v_mfma_f32_16x16x32_bf16 v[60:63], v[196:199], v[124:127], v[60:63]\n"
    "ds_read_b64_tr_b16 v[194:195], v241 offset:36864\n"
    "ds_read_b64_tr_b16 v[196:197], v242 offset:32768\n"
    "; ---- gap 96: QK 8\n"
    "v_mfma_f32_16x16x32_bf16 v[64:67], v[200:203], v[144:147], v[216:219]\n"
    "v_exp_f32_e32 v86, v86\n"
    "v_mfma_f32_16x16x32_bf16 v[72:75], v[200:203], v[148:151], v[220:223]\n"
    "ds_read_b64_tr_b16 v[198:199], v242 offset:36864\n"
    "v_cvt_pk_bf16_f32 v105, v82, v83\n"
    "v_mfma_f32_16x16x32_bf16 v[68:71], v[204:207], v[144:147], v[216:219]\n"
    "v_exp_f32_e32 v87, v87\n"
    "v_mfma_f32_16x16x32_bf16 v[76:79], v[204:207], v[148:151], v[220:223]\n"
    "ds_read_b64_tr_b16 v[200:201], v241 offset:33280\n"
    "ds_read_b64_tr_b16 v[202:203], v241 offset:37376\n"
    "v_cvt_pk_bf16_f32 v106, v84, v85\n"
    "v_mfma_f32_16x16x32_bf16 v[64:67], v[208:211], v[152:155], v[64:67]\n"
    "v_exp_f32_e32 v88, v88\n"
    "v_mfma_f32_16x16x32_bf16 v[72:75], v[208:211], v[156:159], v[72:75]\n"
    "ds_read_b64_tr_b16 v[204:205], v242 offset:33280\n"
    "s_waitcnt lgkmcnt(8)\n"
    "v_mfma_f32_16x16x32_bf16 v[68:71], v[212:215], v[152:155], v[68:71]\n"
    "v_exp_f32_e32 v89, v89\n"
    "v_mfma_f32_16x16x32_bf16 v[76:79], v[212:215], v[156:159], v[76:79]\n"
    "ds_read_b64_tr_b16 v[206:207], v242 offset:37376\n"
    "ds_read_b64_tr_b16 v[208:209], v241 offset:33792\n"
    "v_mfma_f32_16x16x32_bf16 v[64:67], v[176:179], v[160:163], v[64:67]\n"
    "v_exp_f32_e32 v90, v90\n"
    "v_mfma_f32_16x16x32_bf16 v[72:75], v[176:179], v[164:167], v[72:75]\n"
    "ds_read_b64_tr_b16 v[210:211], v241 offset:37888\n"
    "v_cvt_pk_bf16_f32 v107, v86, v87\n"
    "v_mfma_f32_16x16x32_bf16 v[68:71], v[180:183], v[160:163], v[68:71]\n"
    "v_exp_f32_e32 v91, v91\n"
    "v_mfma_f32_16x16x32_bf16 v[76:79], v[180:183], v[164:167], v[76:79]\n"
    "ds_read_b64_tr_b16 v[212:213], v242 offset:33792\n"
    "ds_read_b64_tr_b16 v[214:215], v242 offset:37888\n"
    "v_mfma_f32_16x16x32_bf16 v[64:67], v[184:187], v[168:171], v[64:67]\n"
    "v_exp_f32_e32 v92, v92\n"
    "v_mfma_f32_16x16x32_bf16 v[72:75], v[184:187], v[172:175], v[72:75]\n"
    "ds_read_b64_tr_b16 v[176:177], v241 offset:34304\n"
    "s_waitcnt lgkmcnt(7)\n"
    "v_mfma_f32_16x16x32_bf16 v[68:71], v[188:191], v[168:171], v[68:71]\n"
    "v_exp_f32_e32 v93, v93\n"
    "v_mfma_f32_16x16x32_bf16 v[76:79], v[188:191], v[172:175], v[76:79]\n"
    "ds_read_b64_tr_b16 v[178:179], v241 offset:38400\n"
    "ds_read_b64_tr_b16 v[180:181], v242 offset:34304\n"
    "; ---- gap 104: PV 4\n"
    "v_mfma_f32_16x16x32_bf16 v[0:3], v[192:195], v[128:131], v[0:3]\n"
    "v_cvt_pk_bf16_f32 v108, v88, v89\n"
    "v_cvt_pk_bf16_f32 v109, v90, v91\n"
    "v_mfma_f32_16x16x32_bf16 v[4:7], v[192:195], v[132:135], v[4:7]\n"
    "ds_read_b64_tr_b16 v[182:183], v242 offset:38400\n"
    "v_cvt_pk_bf16_f32 v110, v92, v93\n"
    "v_mfma_f32_16x16x32_bf16 v[8:11], v[196:199], v[128:131], v[8:11]\n"
    "v_exp_f32_e32 v94, v94\n"
    "v_mfma_f32_16x16x32_bf16 v[224:227], v[232:235], v[128:131], v[224:227]\n"
    "v_exp_f32_e32 v95, v95\n"
    "v_mfma_f32_16x16x32_bf16 v[12:15], v[196:199], v[132:135], v[12:15]\n"
    "s_barrier\n"
    "ds_read_b128 v[184:187], v236 offset:8192\n"
    "ds_read_b128 v[188:191], v236 offset:12288\n"
    "v_mfma_f32_16x16x32_bf16 v[16:19], v[200:203], v[128:131], v[16:19]\n"
    "v_exp_f32_e32 v64, v64\n"
    "v_mfma_f32_16x16x32_bf16 v[20:23], v[200:203], v[132:135], v[20:23]\n"
    "ds_read_b128 v[192:195], v237 offset:8192\n"
    "s_waitcnt lgkmcnt(5)\n"
    "v_mfma_f32_16x16x32_bf16 v[24:27], v[204:207], v[128:131], v[24:27]\n"
    "v_exp_f32_e32 v65, v65\n"
    "v_mfma_f32_16x16x32_bf16 v[228:231], v[232:235], v[132:135], v[228:231]\n"
    "v_exp_f32_e32 v66, v66\n"
    "v_mfma_f32_16x16x32_bf16 v[28:31], v[204:207], v[132:135], v[28:31]\n"
    "ds_read_b128 v[196:199], v237 offset:12288\n"
    "ds_read_b128 v[200:203], v238 offset:8192\n"
    "v_mfma_f32_16x16x32_bf16 v[32:35], v[208:211], v[128:131], v[32:35]\n"
    "v_exp_f32_e32 v67, v67\n"
    "v_mfma_f32_16x16x32_bf16 v[36:39], v[208:211], v[132:135], v[36:39]\n"
    "ds_read_b128 v[204:207], v238 offset:12288\n"
    "v_cvt_pk_bf16_f32 v111, v94, v95\n"
    "v_mfma_f32_16x16x32_bf16 v[40:43], v[212:215], v[128:131], v[40:43]\n"
    "v_exp_f32_e32 v68, v68\n"
    "v_mfma_f32_16x16x32_bf16 v[44:47], v[212:215], v[132:135], v[44:47]\n"
    "ds_read_b128 v[208:211], v239 offset:8192\n"
    "ds_read_b128 v[212:215], v239 offset:12288\n"
    "v_mfma_f32_16x16x32_bf16 v[48:51], v[176:179], v[128:131], v[48:51]\n"
    "v_exp_f32_e32 v69, v69\n"
    "v_mfma_f32_16x16x32_bf16 v[52:55], v[176:179], v[132:135], v[52:55]\n"
    "ds_read_b64_tr_b16 v[176:177], v241 offset:40960\n"
    "s_waitcnt lgkmcnt(6)\n"
    "v_mfma_f32_16x16x32_bf16 v[56:59], v[180:183], v[128:131], v[56:59]\n"
    "v_cvt_pk_bf16_f32 v112, v64, v65\n"
    "v_cvt_pk_bf16_f32 v113, v66, v67\n"
    "v_mfma_f32_16x16x32_bf16 v[60:63], v[180:183], v[132:135], v[60:63]\n"
    "ds_read_b64_tr_b16 v[178:179], v241 offset:45056\n"
    "ds_read_b64_tr_b16 v[180:181], v242 offset:40960\n"
    "; ---- gap 112: QK 9\n"
    "v_mfma_f32_16x16x32_bf16 v[80:83], v[184:187], v[144:147], v[216:219]\n"
    "v_exp_f32_e32 v70, v70\n"
    "v_mfma_f32_16x16x32_bf16 v[88:91], v[184:187], v[148:151], v[220:223]\n"
    "ds_read_b64_tr_b16 v[182:183], v242 offset:45056\n"
    "v_cvt_pk_bf16_f32 v114, v68, v69\n"
    "v_mfma_f32_16x16x32_bf16 v[84:87], v[188:191], v[144:147], v[216:219]\n"
    "v_exp_f32_e32 v71, v71\n"
    "v_mfma_f32_16x16x32_bf16 v[92:95], v[188:191], v[148:151], v[220:223]\n"
    "ds_read_b64_tr_b16 v[184:185], v241 offset:41472\n"
    "ds_read_b64_tr_b16 v[186:187], v241 offset:45568\n"
    "v_cvt_pk_bf16_f32 v115, v70, v71\n"
    "v_mfma_f32_16x16x32_bf16 v[80:83], v[192:195], v[152:155], v[80:83]\n"
    "v_exp_f32_e32 v72, v72\n"
    "v_mfma_f32_16x16x32_bf16 v[88:91], v[192:195], v[156:159], v[88:91]\n"
    "ds_read_b64_tr_b16 v[188:189], v242 offset:41472\n"
    "s_waitcnt lgkmcnt(8)\n"
    "v_mfma_f32_16x16x32_bf16 v[84:87], v[196:199], v[152:155], v[84:87]\n"
    "v_exp_f32_e32 v73, v73\n"
    "v_mfma_f32_16x16x32_bf16 v[92:95], v[196:199], v[156:159], v[92:95]\n"
    "ds_read_b64_tr_b16 v[190:191], v242 offset:45568\n"
    "ds_read_b64_tr_b16 v[192:193], v241 offset:41984\n"
    "v_mfma_f32_16x16x32_bf16 v[80:83], v[200:203], v[160:163], v[80:83]\n"
    "v_exp_f32_e32 v74, v74\n"
    "v_mfma_f32_16x16x32_bf16 v[88:91], v[200:203], v[164:167], v[88:91]\n"
    "ds_read_b64_tr_b16 v[194:195], v241 offset:46080\n"
    "v_cvt_pk_bf16_f32 v116, v72, v73\n"
    "v_mfma_f32_16x16x32_bf16 v[84:87], v[204:207], v[160:163], v[84:87]\n"
    "v_exp_f32_e32 v75, v75\n"
    "v_mfma_f32_16x16x32_bf16 v[92:95], v[204:207], v[164:167], v[92:95]\n"
    "ds_read_b64_tr_b16 v[196:197], v242 offset:41984\n"
    "ds_read_b64_tr_b16 v[198:199], v242 offset:46080\n"
    "v_mfma_f32_16x16x32_bf16 v[80:83], v[208:211], v[168:171], v[80:83]\n"
    "v_exp_f32_e32 v76, v76\n"
    "v_mfma_f32_16x16x32_bf16 v[88:91], v[208:211], v[172:175], v[88:91]\n"
    "ds_read_b64_tr_b16 v[200:201], v241 offset:42496\n"
    "s_waitcnt lgkmcnt(7)\n"
    "v_mfma_f32_16x16x32_bf16 v[84:87], v[212:215], v[168:171], v[84:87]\n"
    "v_exp_f32_e32 v77, v77\n"
    "v_mfma_f32_16x16x32_bf16 v[92:95], v[212:215], v[172:175], v[92:95]\n"
    "ds_read_b64_tr_b16 v[202:203], v241 offset:46592\n"
    "ds_read_b64_tr_b16 v[204:205], v242 offset:42496\n"
    "; ---- gap 120: PV 5\n"
    "v_mfma_f32_16x16x32_bf16 v[0:3], v[176:179], v[136:139], v[0:3]\n"
    "v_exp_f32_e32 v78, v78\n"
    "v_mfma_f32_16x16x32_bf16 v[4:7], v[176:179], v[140:143], v[4:7]\n"
    "ds_read_b64_tr_b16 v[206:207], v242 offset:46592\n"
    "v_cvt_pk_bf16_f32 v117, v74, v75\n"
    "v_mfma_f32_16x16x32_bf16 v[8:11], v[180:183], v[136:139], v[8:11]\n"
    "v_exp_f32_e32 v79, v79\n"
    "v_mfma_f32_16x16x32_bf16 v[224:227], v[232:235], v[136:139], v[224:227]\n"
    "v_cvt_pk_bf16_f32 v118, v76, v77\n"
    "v_mfma_f32_16x16x32_bf16 v[12:15], v[180:183], v[140:143], v[12:15]\n"
    "s_barrier\n"
    "ds_read_b128 v[208:211], v236 offset:16384\n"
    "ds_read_b128 v[212:215], v236 offset:20480\n"
    "v_mfma_f32_16x16x32_bf16 v[16:19], v[184:187], v[136:139], v[16:19]\n"
    "v_exp_f32_e32 v80, v80\n"
    "v_mfma_f32_16x16x32_bf16 v[20:23], v[184:187], v[140:143], v[20:23]\n"
    "ds_read_b128 v[176:179], v237 offset:16384\n"
    "s_waitcnt lgkmcnt(5)\n"
    "v_cvt_pk_bf16_f32 v119, v78, v79\n"
    "v_mfma_f32_16x16x32_bf16 v[24:27], v[188:191], v[136:139], v[24:27]\n"
    "v_exp_f32_e32 v81, v81\n"
    "v_mfma_f32_16x16x32_bf16 v[228:231], v[232:235], v[140:143], v[228:231]\n"
    "v_exp_f32_e32 v82, v82\n"
    "v_mfma_f32_16x16x32_bf16 v[28:31], v[188:191], v[140:143], v[28:31]\n"
    "ds_read_b128 v[180:183], v237 offset:20480\n"
    "ds_read_b128 v[184:187], v238 offset:16384\n"
    "v_mfma_f32_16x16x32_bf16 v[32:35], v[192:195], v[136:139], v[32:35]\n"
    "v_exp_f32_e32 v83, v83\n"
    "v_mfma_f32_16x16x32_bf16 v[36:39], v[192:195], v[140:143], v[36:39]\n"
    "ds_read_b128 v[188:191], v238 offset:20480\n"
    "v_cvt_pk_bf16_f32 v120, v80, v81\n"
    "v_mfma_f32_16x16x32_bf16 v[40:43], v[196:199], v[136:139], v[40:43]\n"
    "v_exp_f32_e32 v84, v84\n"
    "v_mfma_f32_16x16x32_bf16 v[44:47], v[196:199], v[140:143], v[44:47]\n"
    "ds_read_b128 v[192:195], v239 offset:16384\n"
    "ds_read_b128 v[196:199], v239 offset:20480\n"
    "v_mfma_f32_16x16x32_bf16 v[48:51], v[200:203], v[136:139], v[48:51]\n"
    "v_exp_f32_e32 v85, v85\n"
    "v_mfma_f32_16x16x32_bf16 v[52:55], v[200:203], v[140:143], v[52:55]\n"
    "ds_read_b64_tr_b16 v[200:201], v241 offset:49152\n"
    "s_waitcnt lgkmcnt(6)\n"
    "v_mfma_f32_16x16x32_bf16 v[56:59], v[204:207], v[136:139], v[56:59]\n"
    "v_exp_f32_e32 v86, v86\n"
    "v_mfma_f32_16x16x32_bf16 v[60:63], v[204:207], v[140:143], v[60:63]\n"
    "ds_read_b64_tr_b16 v[202:203], v241 offset:53248\n"
    "ds_read_b64_tr_b16 v[204:205], v242 offset:49152\n"
    "; ---- gap 128: QK 10\n"
    "v_mfma_f32_16x16x32_bf16 v[64:67], v[208:211], v[144:147], v[216:219]\n"
    "v_exp_f32_e32 v87, v87\n"
    "v_mfma_f32_16x16x32_bf16 v[72:75], v[208:211], v[148:151], v[220:223]\n"
    "ds_read_b64_tr_b16 v[206:207], v242 offset:53248\n"
    "v_cvt_pk_bf16_f32 v121, v82, v83\n"
    "v_mfma_f32_16x16x32_bf16 v[68:71], v[212:215], v[144:147], v[216:219]\n"
    "v_exp_f32_e32 v88, v88\n"
    "v_mfma_f32_16x16x32_bf16 v[76:79], v[212:215], v[148:151], v[220:223]\n"
    "ds_read_b64_tr_b16 v[208:209], v241 offset:49664\n"
    "ds_read_b64_tr_b16 v[210:211], v241 offset:53760\n"
    "v_mfma_f32_16x16x32_bf16 v[64:67], v[176:179], v[152:155], v[64:67]\n"
    "v_exp_f32_e32 v89, v89\n"
    "v_mfma_f32_16x16x32_bf16 v[72:75], v[176:179], v[156:159], v[72:75]\n"
    "ds_read_b64_tr_b16 v[212:213], v242 offset:49664\n"
    "s_waitcnt lgkmcnt(8)\n"
    "v_cvt_pk_bf16_f32 v122, v84, v85\n"
    "v_mfma_f32_16x16x32_bf16 v[68:71], v[180:183], v[152:155], v[68:71]\n"
    "v_exp_f32_e32 v90, v90\n"
    "v_mfma_f32_16x16x32_bf16 v[76:79], v[180:183], v[156:159], v[76:79]\n"
    "ds_read_b64_tr_b16 v[214:215], v242 offset:53760\n"
    "ds_read_b64_tr_b16 v[176:177], v241 offset:50176\n"
    "v_mfma_f32_16x16x32_bf16 v[64:67], v[184:187], v[160:163], v[64:67]\n"
    "v_exp_f32_e32 v91, v91\n"
    "v_mfma_f32_16x16x32_bf16 v[72:75], v[184:187], v[164:167], v[72:75]\n"
    "ds_read_b64_tr_b16 v[178:179], v241 offset:54272\n"
    "v_cvt_pk_bf16_f32 v123, v86, v87\n"
    "v_mfma_f32_16x16x32_bf16 v[68:71], v[188:191], v[160:163], v[68:71]\n"
    "v_exp_f32_e32 v92, v92\n"
    "v_mfma_f32_16x16x32_bf16 v[76:79], v[188:191], v[164:167], v[76:79]\n"
    "ds_read_b64_tr_b16 v[180:181], v242 offset:50176\n"
    "ds_read_b64_tr_b16 v[182:183], v242 offset:54272\n"
    "v_mfma_f32_16x16x32_bf16 v[64:67], v[192:195], v[168:171], v[64:67]\n"
    "v_exp_f32_e32 v93, v93\n"
    "v_mfma_f32_16x16x32_bf16 v[72:75], v[192:195], v[172:175], v[72:75]\n"
    "ds_read_b64_tr_b16 v[184:185], v241 offset:50688\n"
    "s_waitcnt lgkmcnt(7)\n"
    "v_mfma_f32_16x16x32_bf16 v[68:71], v[196:199], v[168:171], v[68:71]\n"
    "v_cvt_pk_bf16_f32 v124, v88, v89\n"
    "v_cvt_pk_bf16_f32 v125, v90, v91\n"
    "v_mfma_f32_16x16x32_bf16 v[76:79], v[196:199], v[172:175], v[76:79]\n"
    "ds_read_b64_tr_b16 v[186:187], v241 offset:54784\n"
    "ds_read_b64_tr_b16 v[188:189], v242 offset:50688\n"
    "; ---- gap 136: PV 6\n"
    "v_mfma_f32_16x16x32_bf16 v[0:3], v[200:203], v[96:99], v[0:3]\n"
    "v_exp_f32_e32 v94, v94\n"
    "v_mfma_f32_16x16x32_bf16 v[4:7], v[200:203], v[100:103], v[4:7]\n"
    "ds_read_b64_tr_b16 v[190:191], v242 offset:54784\n"
    "v_cvt_pk_bf16_f32 v126, v92, v93\n"
    "v_mfma_f32_16x16x32_bf16 v[8:11], v[204:207], v[96:99], v[8:11]\n"
    "v_exp_f32_e32 v95, v95\n"
    "v_mfma_f32_16x16x32_bf16 v[224:227], v[232:235], v[96:99], v[224:227]\n"
    "v_cvt_pk_bf16_f32 v127, v94, v95\n"
    "v_mfma_f32_16x16x32_bf16 v[12:15], v[204:207], v[100:103], v[12:15]\n"
    "s_barrier\n"
    "ds_read_b128 v[192:195], v236 offset:24576\n"
    "ds_read_b128 v[196:199], v236 offset:28672\n"
    "v_mfma_f32_16x16x32_bf16 v[16:19], v[208:211], v[96:99], v[16:19]\n"
    "v_exp_f32_e32 v64, v64\n"
    "v_mfma_f32_16x16x32_bf16 v[20:23], v[208:211], v[100:103], v[20:23]\n"
    "ds_read_b128 v[200:203], v237 offset:24576\n"
    "s_waitcnt lgkmcnt(5)\n"
    "v_mfma_f32_16x16x32_bf16 v[24:27], v[212:215], v[96:99], v[24:27]\n"
    "v_exp_f32_e32 v65, v65\n"
    "v_mfma_f32_16x16x32_bf16 v[228:231], v[232:235], v[100:103], v[228:231]\n"
    "v_exp_f32_e32 v66, v66\n"
    "v_mfma_f32_16x16x32_bf16 v[28:31], v[212:215], v[100:103], v[28:31]\n"
    "ds_read_b128 v[204:207], v237 offset:28672\n"
    "ds_read_b128 v[208:211], v238 offset:24576\n"
    "v_cvt_pk_bf16_f32 v128, v64, v65\n"
    "v_mfma_f32_16x16x32_bf16 v[32:35], v[176:179], v[96:99], v[32:35]\n"
    "v_exp_f32_e32 v67, v67\n"
    "v_mfma_f32_16x16x32_bf16 v[36:39], v[176:179], v[100:103], v[36:39]\n"
    "ds_read_b128 v[212:215], v238 offset:28672\n"
    "v_cvt_pk_bf16_f32 v129, v66, v67\n"
    "v_mfma_f32_16x16x32_bf16 v[40:43], v[180:183], v[96:99], v[40:43]\n"
    "v_exp_f32_e32 v68, v68\n"
    "v_mfma_f32_16x16x32_bf16 v[44:47], v[180:183], v[100:103], v[44:47]\n"
    "ds_read_b128 v[176:179], v239 offset:24576\n"
    "ds_read_b128 v[180:183], v239 offset:28672\n"
    "v_mfma_f32_16x16x32_bf16 v[48:51], v[184:187], v[96:99], v[48:51]\n"
    "v_exp_f32_e32 v69, v69\n"
    "v_mfma_f32_16x16x32_bf16 v[52:55], v[184:187], v[100:103], v[52:55]\n"
    "ds_read_b64_tr_b16 v[184:185], v241 offset:57344\n"
    "s_waitcnt lgkmcnt(6)\n"
    "v_mfma_f32_16x16x32_bf16 v[56:59], v[188:191], v[96:99], v[56:59]\n"
    "v_exp_f32_e32 v70, v70\n"
    "v_mfma_f32_16x16x32_bf16 v[60:63], v[188:191], v[100:103], v[60:63]\n"
    "ds_read_b64_tr_b16 v[186:187], v241 offset:61440\n"
    "ds_read_b64_tr_b16 v[188:189], v242 offset:57344\n"
    "; ---- gap 144: QK 11\n"
    "v_mfma_f32_16x16x32_bf16 v[80:83], v[192:195], v[144:147], v[216:219]\n"
    "v_exp_f32_e32 v71, v71\n"
    "v_mfma_f32_16x16x32_bf16 v[88:91], v[192:195], v[148:151], v[220:223]\n"
    "ds_read_b64_tr_b16 v[190:191], v242 offset:61440\n"
    "v_cvt_pk_bf16_f32 v130, v68, v69\n"
    "v_mfma_f32_16x16x32_bf16 v[84:87], v[196:199], v[144:147], v[216:219]\n"
    "v_exp_f32_e32 v72, v72\n"
    "v_mfma_f32_16x16x32_bf16 v[92:95], v[196:199], v[148:151], v[220:223]\n"
    "ds_read_b64_tr_b16 v[192:193], v241 offset:57856\n"
    "ds_read_b64_tr_b16 v[194:195], v241 offset:61952\n"
    "v_mfma_f32_16x16x32_bf16 v[80:83], v[200:203], v[152:155], v[80:83]\n"
    "v_exp_f32_e32 v73, v73\n"
    "v_mfma_f32_16x16x32_bf16 v[88:91], v[200:203], v[156:159], v[88:91]\n"
    "ds_read_b64_tr_b16 v[196:197], v242 offset:57856\n"
    "s_waitcnt lgkmcnt(8)\n"
    "v_cvt_pk_bf16_f32 v131, v70, v71\n"
    "v_mfma_f32_16x16x32_bf16 v[84:87], v[204:207], v[152:155], v[84:87]\n"
    "v_exp_f32_e32 v74, v74\n"
    "v_mfma_f32_16x16x32_bf16 v[92:95], v[204:207], v[156:159], v[92:95]\n"
    "ds_read_b64_tr_b16 v[198:199], v242 offset:61952\n"
    "ds_read_b64_tr_b16 v[200:201], v241 offset:58368\n"
    "v_mfma_f32_16x16x32_bf16 v[80:83], v[208:211], v[160:163], v[80:83]\n"
    "v_exp_f32_e32 v75, v75\n"
    "v_mfma_f32_16x16x32_bf16 v[88:91], v[208:211], v[164:167], v[88:91]\n"
    "ds_read_b64_tr_b16 v[202:203], v241 offset:62464\n"
    "v_cvt_pk_bf16_f32 v132, v72, v73\n"
    "v_mfma_f32_16x16x32_bf16 v[84:87], v[212:215], v[160:163], v[84:87]\n"
    "v_exp_f32_e32 v76, v76\n"
    "v_mfma_f32_16x16x32_bf16 v[92:95], v[212:215], v[164:167], v[92:95]\n"
    "ds_read_b64_tr_b16 v[204:205], v242 offset:58368\n"
    "ds_read_b64_tr_b16 v[206:207], v242 offset:62464\n"
    "v_mfma_f32_16x16x32_bf16 v[80:83], v[176:179], v[168:171], v[80:83]\n"
    "v_exp_f32_e32 v77, v77\n"
    "v_mfma_f32_16x16x32_bf16 v[88:91], v[176:179], v[172:175], v[88:91]\n"
    "ds_read_b64_tr_b16 v[208:209], v241 offset:58880\n"
    "s_waitcnt lgkmcnt(7)\n"
    "v_mfma_f32_16x16x32_bf16 v[84:87], v[180:183], v[168:171], v[84:87]\n"
    "v_exp_f32_e32 v78, v78\n"
    "v_mfma_f32_16x16x32_bf16 v[92:95], v[180:183], v[172:175], v[92:95]\n"
    "ds_read_b64_tr_b16 v[210:211], v241 offset:62976\n"
    "ds_read_b64_tr_b16 v[212:213], v242 offset:58880\n"
    "; ---- gap 152: PV 7\n"
    "v_mfma_f32_16x16x32_bf16 v[0:3], v[184:187], v[104:107], v[0:3]\n"
    "v_exp_f32_e32 v79, v79\n"
    "v_mfma_f32_16x16x32_bf16 v[4:7], v[184:187], v[108:111], v[4:7]\n"
    "ds_read_b64_tr_b16 v[214:215], v242 offset:62976\n"
    "v_cvt_pk_bf16_f32 v133, v74, v75\n"
    "v_mfma_f32_16x16x32_bf16 v[8:11], v[188:191], v[104:107], v[8:11]\n"
    "v_exp_f32_e32 v80, v80\n"
    "v_mfma_f32_16x16x32_bf16 v[224:227], v[232:235], v[104:107], v[224:227]\n"
    "v_cvt_pk_bf16_f32 v134, v76, v77\n"
    "v_mfma_f32_16x16x32_bf16 v[12:15], v[188:191], v[108:111], v[12:15]\n"
    "s_barrier\n"
    "ds_read_b128 v[176:179], v236 offset:32768\n"
    "ds_read_b128 v[180:183], v236 offset:36864\n"
    "v_mfma_f32_16x16x32_bf16 v[16:19], v[192:195], v[104:107], v[16:19]\n"
    "v_exp_f32_e32 v81, v81\n"
    "v_mfma_f32_16x16x32_bf16 v[20:23], v[192:195], v[108:111], v[20:23]\n"
    "ds_read_b128 v[184:187], v237 offset:32768\n"
    "s_waitcnt lgkmcnt(5)\n"
    "v_mfma_f32_16x16x32_bf16 v[24:27], v[196:199], v[104:107], v[24:27]\n"
    "v_exp_f32_e32 v82, v82\n"
    "v_mfma_f32_16x16x32_bf16 v[228:231], v[232:235], v[108:111], v[228:231]\n"
    "v_exp_f32_e32 v83, v83\n"
    "v_mfma_f32_16x16x32_bf16 v[28:31], v[196:199], v[108:111], v[28:31]\n"
    "ds_read_b128 v[188:191], v237 offset:36864\n"
    "ds_read_b128 v[192:195], v238 offset:32768\n"
    "v_cvt_pk_bf16_f32 v135, v78, v79\n"
    "v_mfma_f32_16x16x32_bf16 v[32:35], v[200:203], v[104:107], v[32:35]\n"
    "v_exp_f32_e32 v84, v84\n"
    "v_mfma_f32_16x16x32_bf16 v[36:39], v[200:203], v[108:111], v[36:39]\n"
    "ds_read_b128 v[196:199], v238 offset:36864\n"
    "v_cvt_pk_bf16_f32 v136, v80, v81\n"
    "v_mfma_f32_16x16x32_bf16 v[40:43], v[204:207], v[104:107], v[40:43]\n"
    "v_exp_f32_e32 v85, v85\n"
    "v_mfma_f32_16x16x32_bf16 v[44:47], v[204:207], v[108:111], v[44:47]\n"
    "ds_read_b128 v[200:203], v239 offset:32768\n"
    "ds_read_b128 v[204:207], v239 offset:36864\n"
    "v_mfma_f32_16x16x32_bf16 v[48:51], v[208:211], v[104:107], v[48:51]\n"
    "v_exp_f32_e32 v86, v86\n"
    "v_mfma_f32_16x16x32_bf16 v[52:55], v[208:211], v[108:111], v[52:55]\n"
    "ds_read_b64_tr_b16 v[208:209], v241 offset:0\n"
    "s_waitcnt lgkmcnt(6)\n"
    "v_mfma_f32_16x16x32_bf16 v[56:59], v[212:215], v[104:107], v[56:59]\n"
    "v_exp_f32_e32 v87, v87\n"
    "v_mfma_f32_16x16x32_bf16 v[60:63], v[212:215], v[108:111], v[60:63]\n"
    "ds_read_b64_tr_b16 v[210:211], v241 offset:4096\n"
    "ds_read_b64_tr_b16 v[212:213], v242 offset:0\n"
    "; ---- gap 160: QK 12\n"
    "v_mfma_f32_16x16x32_bf16 v[64:67], v[176:179], v[144:147], v[216:219]\n"
    "v_cvt_pk_bf16_f32 v137, v82, v83\n"
    "v_cvt_pk_bf16_f32 v138, v84, v85\n"
    "v_mfma_f32_16x16x32_bf16 v[72:75], v[176:179], v[148:151], v[220:223]\n"
    "ds_read_b64_tr_b16 v[214:215], v242 offset:4096\n"
    "v_cvt_pk_bf16_f32 v139, v86, v87\n"
    "v_mfma_f32_16x16x32_bf16 v[68:71], v[180:183], v[144:147], v[216:219]\n"
    "v_exp_f32_e32 v88, v88\n"
    "v_mfma_f32_16x16x32_bf16 v[76:79], v[180:183], v[148:151], v[220:223]\n"
    "ds_read_b64_tr_b16 v[176:177], v241 offset:512\n"
    "ds_read_b64_tr_b16 v[178:179], v241 offset:4608\n"
    "v_mfma_f32_16x16x32_bf16 v[64:67], v[184:187], v[152:155], v[64:67]\n"
    "v_exp_f32_e32 v89, v89\n"
    "v_mfma_f32_16x16x32_bf16 v[72:75], v[184:187], v[156:159], v[72:75]\n"
    "ds_read_b64_tr_b16 v[180:181], v242 offset:512\n"
    "s_waitcnt lgkmcnt(8)\n"
    "v_mfma_f32_16x16x32_bf16 v[68:71], v[188:191], v[152:155], v[68:71]\n"
    "v_exp_f32_e32 v90, v90\n"
    "v_mfma_f32_16x16x32_bf16 v[76:79], v[188:191], v[156:159], v[76:79]\n"
    "ds_read_b64_tr_b16 v[182:183], v242 offset:4608\n"
    "ds_read_b64_tr_b16 v[184:185], v241 offset:1024\n"
    "v_cvt_pk_bf16_f32 v140, v88, v89\n"
    "v_mfma_f32_16x16x32_bf16 v[64:67], v[192:195], v[160:163], v[64:67]\n"
    "v_exp_f32_e32 v91, v91\n"
    "v_mfma_f32_16x16x32_bf16 v[72:75], v[192:195], v[164:167], v[72:75]\n"
    "ds_read_b64_tr_b16 v[186:187], v241 offset:5120\n"
    "v_cvt_pk_bf16_f32 v141, v90, v91\n"
    "v_mfma_f32_16x16x32_bf16 v[68:71], v[196:199], v[160:163], v[68:71]\n"
    "v_exp_f32_e32 v92, v92\n"
    "v_mfma_f32_16x16x32_bf16 v[76:79], v[196:199], v[164:167], v[76:79]\n"
    "ds_read_b64_tr_b16 v[188:189], v242 offset:1024\n"
    "ds_read_b64_tr_b16 v[190:191], v242 offset:5120\n"
    "v_mfma_f32_16x16x32_bf16 v[64:67], v[200:203], v[168:171], v[64:67]\n"
    "v_exp_f32_e32 v93, v93\n"
    "v_mfma_f32_16x16x32_bf16 v[72:75], v[200:203], v[172:175], v[72:75]\n"
    "ds_read_b64_tr_b16 v[192:193], v241 offset:1536\n"
    "s_waitcnt lgkmcnt(7)\n"
    "v_mfma_f32_16x16x32_bf16 v[68:71], v[204:207], v[168:171], v[68:71]\n"
    "v_exp_f32_e32 v94, v94\n"
    "v_mfma_f32_16x16x32_bf16 v[76:79], v[204:207], v[172:175], v[76:79]\n"
    "ds_read_b64_tr_b16 v[194:195], v241 offset:5632\n"
    "ds_read_b64_tr_b16 v[196:197], v242 offset:1536\n"
    "; ---- gap 168: PV 8\n"
    "v_mfma_f32_16x16x32_bf16 v[0:3], v[208:211], v[112:115], v[0:3]\n"
    "v_exp_f32_e32 v95, v95\n"
    "v_mfma_f32_16x16x32_bf16 v[4:7], v[208:211], v[116:119], v[4:7]\n"
    "ds_read_b64_tr_b16 v[198:199], v242 offset:5632\n"
    "v_cvt_pk_bf16_f32 v142, v92, v93\n"
    "v_mfma_f32_16x16x32_bf16 v[8:11], v[212:215], v[112:115], v[8:11]\n"
    "v_exp_f32_e32 v64, v64\n"
    "v_mfma_f32_16x16x32_bf16 v[224:227], v[232:235], v[112:115], v[224:227]\n"
    "v_cvt_pk_bf16_f32 v143, v94, v95\n"
    "v_mfma_f32_16x16x32_bf16 v[12:15], v[212:215], v[116:119], v[12:15]\n"
    "s_barrier\n"
    "ds_read_b128 v[200:203], v236 offset:40960\n"
    "ds_read_b128 v[204:207], v236 offset:45056\n"
    "v_mfma_f32_16x16x32_bf16 v[16:19], v[176:179], v[112:115], v[16:19]\n"
    "v_exp_f32_e32 v65, v65\n"
    "v_mfma_f32_16x16x32_bf16 v[20:23], v[176:179], v[116:119], v[20:23]\n"
    "ds_read_b128 v[208:211], v237 offset:40960\n"
    "s_waitcnt lgkmcnt(5)\n"
    "v_mfma_f32_16x16x32_bf16 v[24:27], v[180:183], v[112:115], v[24:27]\n"
    "v_exp_f32_e32 v66, v66\n"
    "v_mfma_f32_16x16x32_bf16 v[228:231], v[232:235], v[116:119], v[228:231]\n"
    "v_exp_f32_e32 v67, v67\n"
    "v_mfma_f32_16x16x32_bf16 v[28:31], v[180:183], v[116:119], v[28:31]\n"
    "ds_read_b128 v[212:215], v237 offset:45056\n"
    "ds_read_b128 v[176:179], v238 offset:40960\n"
    "v_cvt_pk_bf16_f32 v96, v64, v65\n"
    "v_mfma_f32_16x16x32_bf16 v[32:35], v[184:187], v[112:115], v[32:35]\n"
    "v_exp_f32_e32 v68, v68\n"
    "v_mfma_f32_16x16x32_bf16 v[36:39], v[184:187], v[116:119], v[36:39]\n"
    "ds_read_b128 v[180:183], v238 offset:45056\n"
    "v_cvt_pk_bf16_f32 v97, v66, v67\n"
    "v_mfma_f32_16x16x32_bf16 v[40:43], v[188:191], v[112:115], v[40:43]\n"
    "v_exp_f32_e32 v69, v69\n"
    "v_mfma_f32_16x16x32_bf16 v[44:47], v[188:191], v[116:119], v[44:47]\n"
    "ds_read_b128 v[184:187], v239 offset:40960\n"
    "ds_read_b128 v[188:191], v239 offset:45056\n"
    "v_mfma_f32_16x16x32_bf16 v[48:51], v[192:195], v[112:115], v[48:51]\n"
    "v_exp_f32_e32 v70, v70\n"
    "v_mfma_f32_16x16x32_bf16 v[52:55], v[192:195], v[116:119], v[52:55]\n"
    "ds_read_b64_tr_b16 v[192:193], v241 offset:8192\n"
    "s_waitcnt lgkmcnt(6)\n"
    "v_mfma_f32_16x16x32_bf16 v[56:59], v[196:199], v[112:115], v[56:59]\n"
    "v_exp_f32_e32 v71, v71\n"
    "v_mfma_f32_16x16x32_bf16 v[60:63], v[196:199], v[116:119], v[60:63]\n"
    "ds_read_b64_tr_b16 v[194:195], v241 offset:12288\n"
    "ds_read_b64_tr_b16 v[196:197], v242 offset:8192\n"
    "; ---- gap 176: QK 13\n"
    "v_mfma_f32_16x16x32_bf16 v[80:83], v[200:203], v[144:147], v[216:219]\n"
    "v_exp_f32_e32 v72, v72\n"
    "v_mfma_f32_16x16x32_bf16 v[88:91], v[200:203], v[148:151], v[220:223]\n"
    "ds_read_b64_tr_b16 v[198:199], v242 offset:12288\n"
    "v_cvt_pk_bf16_f32 v98, v68, v69\n"
    "v_mfma_f32_16x16x32_bf16 v[84:87], v[204:207], v[144:147], v[216:219]\n"
    "v_exp_f32_e32 v73, v73\n"
    "v_mfma_f32_16x16x32_bf16 v[92:95], v[204:207], v[148:151], v[220:223]\n"
    "ds_read_b64_tr_b16 v[200:201], v241 offset:8704\n"
    "ds_read_b64_tr_b16 v[202:203], v241 offset:12800\n"
    "v_mfma_f32_16x16x32_bf16 v[80:83], v[208:211], v[152:155], v[80:83]\n"
    "v_exp_f32_e32 v74, v74\n"
    "v_mfma_f32_16x16x32_bf16 v[88:91], v[208:211], v[156:159], v[88:91]\n"
    "ds_read_b64_tr_b16 v[204:205], v242 offset:8704\n"
    "s_waitcnt lgkmcnt(8)\n"
    "v_mfma_f32_16x16x32_bf16 v[84:87], v[212:215], v[152:155], v[84:87]\n"
    "v_exp_f32_e32 v75, v75\n"
    "v_mfma_f32_16x16x32_bf16 v[92:95], v[212:215], v[156:159], v[92:95]\n"
    "ds_read_b64_tr_b16 v[206:207], v242 offset:12800\n"
    "ds_read_b64_tr_b16 v[208:209], v241 offset:9216\n"
    "v_cvt_pk_bf16_f32 v99, v70, v71\n"
    "v_mfma_f32_16x16x32_bf16 v[80:83], v[176:179], v[160:163], v[80:83]\n"
    "v_exp_f32_e32 v76, v76\n"
    "v_mfma_f32_16x16x32_bf16 v[88:91], v[176:179], v[164:167], v[88:91]\n"
    "ds_read_b64_tr_b16 v[210:211], v241 offset:13312\n"
    "v_cvt_pk_bf16_f32 v100, v72, v73\n"
    "v_mfma_f32_16x16x32_bf16 v[84:87], v[180:183], v[160:163], v[84:87]\n"
    "v_exp_f32_e32 v77, v77\n"
    "v_mfma_f32_16x16x32_bf16 v[92:95], v[180:183], v[164:167], v[92:95]\n"
    "ds_read_b64_tr_b16 v[212:213], v242 offset:9216\n"
    "ds_read_b64_tr_b16 v[214:215], v242 offset:13312\n"
    "v_mfma_f32_16x16x32_bf16 v[80:83], v[184:187], v[168:171], v[80:83]\n"
    "v_exp_f32_e32 v78, v78\n"
    "v_mfma_f32_16x16x32_bf16 v[88:91], v[184:187], v[172:175], v[88:91]\n"
    "ds_read_b64_tr_b16 v[176:177], v241 offset:9728\n"
    "s_waitcnt lgkmcnt(7)\n"
    "v_mfma_f32_16x16x32_bf16 v[84:87], v[188:191], v[168:171], v[84:87]\n"
    "v_exp_f32_e32 v79, v79\n"
    "v_mfma_f32_16x16x32_bf16 v[92:95], v[188:191], v[172:175], v[92:95]\n"
    "ds_read_b64_tr_b16 v[178:179], v241 offset:13824\n"
    "ds_read_b64_tr_b16 v[180:181], v242 offset:9728\n"
    "; ---- gap 184: PV 9\n"
    "v_mfma_f32_16x16x32_bf16 v[0:3], v[192:195], v[120:123], v[0:3]\n"
    "v_cvt_pk_bf16_f32 v101, v74, v75\n"
    "v_cvt_pk_bf16_f32 v102, v76, v77\n"
    "v_mfma_f32_16x16x32_bf16 v[4:7], v[192:195], v[124:127], v[4:7]\n"
    "ds_read_b64_tr_b16 v[182:183], v242 offset:13824\n"
    "v_cvt_pk_bf16_f32 v103, v78, v79\n"
    "v_mfma_f32_16x16x32_bf16 v[8:11], v[196:199], v[120:123], v[8:11]\n"
    "v_exp_f32_e32 v80, v80\n"
    "v_mfma_f32_16x16x32_bf16 v[224:227], v[232:235], v[120:123], v[224:227]\n"
    "v_mfma_f32_16x16x32_bf16 v[12:15], v[196:199], v[124:127], v[12:15]\n"
    "s_barrier\n"
    "ds_read_b128 v[184:187], v236 offset:49152\n"
    "ds_read_b128 v[188:191], v236 offset:53248\n"
    "v_mfma_f32_16x16x32_bf16 v[16:19], v[200:203], v[120:123], v[16:19]\n"
    "v_exp_f32_e32 v81, v81\n"
    "v_mfma_f32_16x16x32_bf16 v[20:23], v[200:203], v[124:127], v[20:23]\n"
    "ds_read_b128 v[192:195], v237 offset:49152\n"
    "s_waitcnt lgkmcnt(5)\n"
    "v_cvt_pk_bf16_f32 v104, v80, v81\n"
    "v_mfma_f32_16x16x32_bf16 v[24:27], v[204:207], v[120:123], v[24:27]\n"
    "v_exp_f32_e32 v82, v82\n"
    "v_mfma_f32_16x16x32_bf16 v[228:231], v[232:235], v[124:127], v[228:231]\n"
    "v_exp_f32_e32 v83, v83\n"
    "v_mfma_f32_16x16x32_bf16 v[28:31], v[204:207], v[124:127], v[28:31]\n"
    "ds_read_b128 v[196:199], v237 offset:53248\n"
    "ds_read_b128 v[200:203], v238 offset:49152\n"
    "v_mfma_f32_16x16x32_bf16 v[32:35], v[208:211], v[120:123], v[32:35]\n"
    "v_exp_f32_e32 v84, v84\n"
    "v_mfma_f32_16x16x32_bf16 v[36:39], v[208:211], v[124:127], v[36:39]\n"
    "ds_read_b128 v[204:207], v238 offset:53248\n"
    "v_cvt_pk_bf16_f32 v105, v82, v83\n"
    "v_mfma_f32_16x16x32_bf16 v[40:43], v[212:215], v[120:123], v[40:43]\n"
    "v_exp_f32_e32 v85, v85\n"
    "v_mfma_f32_16x16x32_bf16 v[44:47], v[212:215], v[124:127], v[44:47]\n"
    "ds_read_b128 v[208:211], v239 offset:49152\n"
    "ds_read_b128 v[212:215], v239 offset:53248\n"
    "v_cvt_pk_bf16_f32 v106, v84, v85\n"
    "v_mfma_f32_16x16x32_bf16 v[48:51], v[176:179], v[120:123], v[48:51]\n"
    "v_exp_f32_e32 v86, v86\n"
    "v_mfma_f32_16x16x32_bf16 v[52:55], v[176:179], v[124:127], v[52:55]\n"
    "ds_read_b64_tr_b16 v[176:177], v241 offset:16384\n"
    "s_waitcnt lgkmcnt(6)\n"
    "v_mfma_f32_16x16x32_bf16 v[56:59], v[180:183], v[120:123], v[56:59]\n"
    "v_exp_f32_e32 v87, v87\n"
    "v_mfma_f32_16x16x32_bf16 v[60:63], v[180:183], v[124:127], v[60:63]\n"
    "ds_read_b64_tr_b16 v[178:179], v241 offset:20480\n"
    "ds_read_b64_tr_b16 v[180:181], v242 offset:16384\n"
    "; ---- gap 192: QK 14\n"
    "v_mfma_f32_16x16x32_bf16 v[64:67], v[184:187], v[144:147], v[216:219]\n"
    "v_exp_f32_e32 v88, v88\n"
    "v_mfma_f32_16x16x32_bf16 v[72:75], v[184:187], v[148:151], v[220:223]\n"
    "ds_read_b64_tr_b16 v[182:183], v242 offset:20480\n"
    "v_cvt_pk_bf16_f32 v107, v86, v87\n"
    "v_mfma_f32_16x16x32_bf16 v[68:71], v[188:191], v[144:147], v[216:219]\n"
    "v_exp_f32_e32 v89, v89\n"
    "v_mfma_f32_16x16x32_bf16 v[76:79], v[188:191], v[148:151], v[220:223]\n"
    "ds_read_b64_tr_b16 v[184:185], v241 offset:16896\n"
    "ds_read_b64_tr_b16 v[186:187], v241 offset:20992\n"
    "v_mfma_f32_16x16x32_bf16 v[64:67], v[192:195], v[152:155], v[64:67]\n"
    "v_exp_f32_e32 v90, v90\n"
    "v_mfma_f32_16x16x32_bf16 v[72:75], v[192:195], v[156:159], v[72:75]\n"
    "ds_read_b64_tr_b16 v[188:189], v242 offset:16896\n"
    "s_waitcnt lgkmcnt(8)\n"
    "v_mfma_f32_16x16x32_bf16 v[68:71], v[196:199], v[152:155], v[68:71]\n"
    "v_exp_f32_e32 v91, v91\n"
    "v_mfma_f32_16x16x32_bf16 v[76:79], v[196:199], v[156:159], v[76:79]\n"
    "ds_read_b64_tr_b16 v[190:191], v242 offset:20992\n"
    "ds_read_b64_tr_b16 v[192:193], v241 offset:17408\n"
    "v_mfma_f32_16x16x32_bf16 v[64:67], v[200:203], v[160:163], v[64:67]\n"
    "v_exp_f32_e32 v92, v92\n"
    "v_mfma_f32_16x16x32_bf16 v[72:75], v[200:203], v[164:167], v[72:75]\n"
    "ds_read_b64_tr_b16 v[194:195], v241 offset:21504\n"
    "v_cvt_pk_bf16_f32 v108, v88, v89\n"
    "v_cvt_pk_bf16_f32 v109, v90, v91\n"
    "v_mfma_f32_16x16x32_bf16 v[68:71], v[204:207], v[160:163], v[68:71]\n"
    "v_exp_f32_e32 v93, v93\n"
    "v_mfma_f32_16x16x32_bf16 v[76:79], v[204:207], v[164:167], v[76:79]\n"
    "ds_read_b64_tr_b16 v[196:197], v242 offset:17408\n"
    "ds_read_b64_tr_b16 v[198:199], v242 offset:21504\n"
    "v_mfma_f32_16x16x32_bf16 v[64:67], v[208:211], v[168:171], v[64:67]\n"
    "v_exp_f32_e32 v94, v94\n"
    "v_mfma_f32_16x16x32_bf16 v[72:75], v[208:211], v[172:175], v[72:75]\n"
    "ds_read_b64_tr_b16 v[200:201], v241 offset:17920\n"
    "s_waitcnt lgkmcnt(7)\n"
    "v_mfma_f32_16x16x32_bf16 v[68:71], v[212:215], v[168:171], v[68:71]\n"
    "v_exp_f32_e32 v95, v95\n"
    "v_mfma_f32_16x16x32_bf16 v[76:79], v[212:215], v[172:175], v[76:79]\n"
    "ds_read_b64_tr_b16 v[202:203], v241 offset:22016\n"
    "ds_read_b64_tr_b16 v[204:205], v242 offset:17920\n"
    "; ---- gap 200: PV 10\n"
    "v_mfma_f32_16x16x32_bf16 v[0:3], v[176:179], v[128:131], v[0:3]\n"
    "v_cvt_pk_bf16_f32 v110, v92, v93\n"
    "v_cvt_pk_bf16_f32 v111, v94, v95\n"
    "v_mfma_f32_16x16x32_bf16 v[4:7], v[176:179], v[132:135], v[4:7]\n"
    "ds_read_b64_tr_b16 v[206:207], v242 offset:22016\n"
    "v_mfma_f32_16x16x32_bf16 v[8:11], v[180:183], v[128:131], v[8:11]\n"
    "v_exp_f32_e32 v64, v64\n"
    "v_mfma_f32_16x16x32_bf16 v[224:227], v[232:235], v[128:131], v[224:227]\n"
    "v_exp_f32_e32 v65, v65\n"
    "v_mfma_f32_16x16x32_bf16 v[12:15], v[180:183], v[132:135], v[12:15]\n"
    "s_barrier\n"
    "ds_read_b128 v[208:211], v236 offset:57344\n"
    "ds_read_b128 v[212:215], v236 offset:61440\n"
    "v_mfma_f32_16x16x32_bf16 v[16:19], v[184:187], v[128:131], v[16:19]\n"
    "v_exp_f32_e32 v66, v66\n"
    "v_mfma_f32_16x16x32_bf16 v[20:23], v[184:187], v[132:135], v[20:23]\n"
    "ds_read_b128 v[176:179], v237 offset:57344\n"
    "s_waitcnt lgkmcnt(5)\n"
    "v_mfma_f32_16x16x32_bf16 v[24:27], v[188:191], v[128:131], v[24:27]\n"
    "v_exp_f32_e32 v67, v67\n"
    "v_mfma_f32_16x16x32_bf16 v[228:231], v[232:235], v[132:135], v[228:231]\n"
    "v_exp_f32_e32 v68, v68\n"
    "v_mfma_f32_16x16x32_bf16 v[28:31], v[188:191], v[132:135], v[28:31]\n"
    "ds_read_b128 v[180:183], v237 offset:61440\n"
    "ds_read_b128 v[184:187], v238 offset:57344\n"
    "v_mfma_f32_16x16x32_bf16 v[32:35], v[192:195], v[128:131], v[32:35]\n"
    "v_exp_f32_e32 v69, v69\n"
    "v_mfma_f32_16x16x32_bf16 v[36:39], v[192:195], v[132:135], v[36:39]\n"
    "ds_read_b128 v[188:191], v238 offset:61440\n"
    "v_cvt_pk_bf16_f32 v112, v64, v65\n"
    "v_cvt_pk_bf16_f32 v113, v66, v67\n"
    "v_mfma_f32_16x16x32_bf16 v[40:43], v[196:199], v[128:131], v[40:43]\n"
    "v_exp_f32_e32 v70, v70\n"
    "v_mfma_f32_16x16x32_bf16 v[44:47], v[196:199], v[132:135], v[44:47]\n"
    "ds_read_b128 v[192:195], v239 offset:57344\n"
    "ds_read_b128 v[196:199], v239 offset:61440\n"
    "v_mfma_f32_16x16x32_bf16 v[48:51], v[200:203], v[128:131], v[48:51]\n"
    "v_exp_f32_e32 v71, v71\n"
    "v_mfma_f32_16x16x32_bf16 v[52:55], v[200:203], v[132:135], v[52:55]\n"
    "ds_read_b64_tr_b16 v[200:201], v241 offset:24576\n"
    "s_waitcnt lgkmcnt(6)\n"
    "v_mfma_f32_16x16x32_bf16 v[56:59], v[204:207], v[128:131], v[56:59]\n"
    "v_exp_f32_e32 v72, v72\n"
    "v_mfma_f32_16x16x32_bf16 v[60:63], v[204:207], v[132:135], v[60:63]\n"
    "ds_read_b64_tr_b16 v[202:203], v241 offset:28672\n"
    "ds_read_b64_tr_b16 v[204:205], v242 offset:24576\n"
    "; ---- gap 208: QK 15\n"
    "v_mfma_f32_16x16x32_bf16 v[80:83], v[208:211], v[144:147], v[216:219]\n"
    "v_exp_f32_e32 v73, v73\n"
    "v_mfma_f32_16x16x32_bf16 v[88:91], v[208:211], v[148:151], v[220:223]\n"
    "ds_read_b64_tr_b16 v[206:207], v242 offset:28672\n"
    "v_cvt_pk_bf16_f32 v114, v68, v69\n"
    "v_mfma_f32_16x16x32_bf16 v[84:87], v[212:215], v[144:147], v[216:219]\n"
    "v_exp_f32_e32 v74, v74\n"
    "v_mfma_f32_16x16x32_bf16 v[224:227], v[232:235], v[104:107], v[224:227]\n"
    "v_exp_f32_e32 v75, v75\n"
    "v_mfma_f32_16x16x32_bf16 v[92:95], v[212:215], v[148:151], v[220:223]\n"
    "ds_read_b64_tr_b16 v[208:209], v241 offset:25088\n"
    "ds_read_b64_tr_b16 v[210:211], v241 offset:29184\n"
    "v_mfma_f32_16x16x32_bf16 v[80:83], v[176:179], v[152:155], v[80:83]\n"
    "v_cvt_pk_bf16_f32 v115, v70, v71\n"
    "v_cvt_pk_bf16_f32 v116, v72, v73\n"
    "v_mfma_f32_16x16x32_bf16 v[88:91], v[176:179], v[156:159], v[88:91]\n"
    "ds_read_b64_tr_b16 v[212:213], v242 offset:25088\n"
    "s_waitcnt lgkmcnt(8)\n"
    "v_mfma_f32_16x16x32_bf16 v[84:87], v[180:183], v[152:155], v[84:87]\n"
    "v_exp_f32_e32 v76, v76\n"
    "v_mfma_f32_16x16x32_bf16 v[228:231], v[232:235], v[108:111], v[228:231]\n"
    "v_exp_f32_e32 v77, v77\n"
    "v_mfma_f32_16x16x32_bf16 v[92:95], v[180:183], v[156:159], v[92:95]\n"
    "ds_read_b64_tr_b16 v[214:215], v242 offset:29184\n"
    "ds_read_b64_tr_b16 v[176:177], v241 offset:25600\n"
    "v_mfma_f32_16x16x32_bf16 v[80:83], v[184:187], v[160:163], v[80:83]\n"
    "v_exp_f32_e32 v78, v78\n"
    "v_mfma_f32_16x16x32_bf16 v[88:91], v[184:187], v[164:167], v[88:91]\n"
    "ds_read_b64_tr_b16 v[178:179], v241 offset:29696\n"
    "v_cvt_pk_bf16_f32 v117, v74, v75\n"
    "v_cvt_pk_bf16_f32 v118, v76, v77\n"
    "v_mfma_f32_16x16x32_bf16 v[84:87], v[188:191], v[160:163], v[84:87]\n"
    "v_exp_f32_e32 v79, v79\n"
    "v_mfma_f32_16x16x32_bf16 v[92:95], v[188:191], v[164:167], v[92:95]\n"
    "ds_read_b64_tr_b16 v[180:181], v242 offset:25600\n"
    "ds_read_b64_tr_b16 v[182:183], v242 offset:29696\n"
    "v_mfma_f32_16x16x32_bf16 v[80:83], v[192:195], v[168:171], v[80:83]\n"
    "v_cvt_pk_bf16_f32 v119, v78, v79\n"
    "v_mfma_f32_16x16x32_bf16 v[88:91], v[192:195], v[172:175], v[88:91]\n"
    "ds_read_b64_tr_b16 v[184:185], v241 offset:26112\n"
    "s_waitcnt lgkmcnt(7)\n"
    "v_mfma_f32_16x16x32_bf16 v[84:87], v[196:199], v[168:171], v[84:87]\n"
    "v_mfma_f32_16x16x32_bf16 v[92:95], v[196:199], v[172:175], v[92:95]\n"
    "ds_read_b64_tr_b16 v[186:187], v241 offset:30208\n"
    "ds_read_b64_tr_b16 v[188:189], v242 offset:26112\n"
    "; ---- gap 216: PV 11\n"
    "v_mfma_f32_16x16x32_bf16 v[0:3], v[200:203], v[136:139], v[0:3]\n"
    "v_mfma_f32_16x16x32_bf16 v[4:7], v[200:203], v[140:143], v[4:7]\n"
    "ds_read_b64_tr_b16 v[190:191], v242 offset:30208\n"
    "v_mfma_f32_16x16x32_bf16 v[8:11], v[204:207], v[136:139], v[8:11]\n"
    "v_exp_f32_e32 v80, v80\n"
    "v_mfma_f32_16x16x32_bf16 v[224:227], v[232:235], v[136:139], v[224:227]\n"
    "v_exp_f32_e32 v81, v81\n"
    "v_mfma_f32_16x16x32_bf16 v[12:15], v[204:207], v[140:143], v[12:15]\n"
    "ds_read_b64_tr_b16 v[192:193], v241 offset:32768\n"
    "ds_read_b64_tr_b16 v[194:195], v241 offset:36864\n"
    "v_cvt_pk_bf16_f32 v120, v80, v81\n"
    "v_mfma_f32_16x16x32_bf16 v[16:19], v[208:211], v[136:139], v[16:19]\n"
    "v_exp_f32_e32 v82, v82\n"
    "v_mfma_f32_16x16x32_bf16 v[20:23], v[208:211], v[140:143], v[20:23]\n"
    "ds_read_b64_tr_b16 v[196:197], v242 offset:32768\n"
    "s_waitcnt lgkmcnt(5)\n"
    "v_mfma_f32_16x16x32_bf16 v[24:27], v[212:215], v[136:139], v[24:27]\n"
    "v_exp_f32_e32 v83, v83\n"
    "v_mfma_f32_16x16x32_bf16 v[228:231], v[232:235], v[140:143], v[228:231]\n"
    "v_exp_f32_e32 v84, v84\n"
    "v_mfma_f32_16x16x32_bf16 v[28:31], v[212:215], v[140:143], v[28:31]\n"
    "ds_read_b64_tr_b16 v[198:199], v242 offset:36864\n"
    "ds_read_b64_tr_b16 v[200:201], v241 offset:33280\n"
    "v_cvt_pk_bf16_f32 v121, v82, v83\n"
    "v_mfma_f32_16x16x32_bf16 v[32:35], v[176:179], v[136:139], v[32:35]\n"
    "v_exp_f32_e32 v85, v85\n"
    "v_mfma_f32_16x16x32_bf16 v[36:39], v[176:179], v[140:143], v[36:39]\n"
    "ds_read_b64_tr_b16 v[202:203], v241 offset:37376\n"
    "v_cvt_pk_bf16_f32 v122, v84, v85\n"
    "v_mfma_f32_16x16x32_bf16 v[40:43], v[180:183], v[136:139], v[40:43]\n"
    "v_exp_f32_e32 v86, v86\n"
    "v_mfma_f32_16x16x32_bf16 v[44:47], v[180:183], v[140:143], v[44:47]\n"
    "ds_read_b64_tr_b16 v[204:205], v242 offset:33280\n"
    "ds_read_b64_tr_b16 v[206:207], v242 offset:37376\n"
    "v_exp_f32_e32 v87, v87\n"
    "v_mfma_f32_16x16x32_bf16 v[48:51], v[184:187], v[136:139], v[48:51]\n"
    "v_exp_f32_e32 v88, v88\n"
    "v_mfma_f32_16x16x32_bf16 v[52:55], v[184:187], v[140:143], v[52:55]\n"
    "ds_read_b64_tr_b16 v[208:209], v241 offset:33792\n"
    "s_waitcnt lgkmcnt(5)\n"
    "v_mfma_f32_16x16x32_bf16 v[56:59], v[188:191], v[136:139], v[56:59]\n"
    "v_exp_f32_e32 v89, v89\n"
    "v_mfma_f32_16x16x32_bf16 v[60:63], v[188:191], v[140:143], v[60:63]\n"
    "ds_read_b64_tr_b16 v[210:211], v241 offset:37888\n"
    "ds_read_b64_tr_b16 v[212:213], v242 offset:33792\n"
    "v_cvt_pk_bf16_f32 v123, v86, v87\n"
    "; ---- gap 224: PV 12\n"
    "v_mfma_f32_16x16x32_bf16 v[0:3], v[192:195], v[96:99], v[0:3]\n"
    "v_exp_f32_e32 v90, v90\n"
    "v_mfma_f32_16x16x32_bf16 v[4:7], v[192:195], v[100:103], v[4:7]\n"
    "ds_read_b64_tr_b16 v[214:215], v242 offset:37888\n"
    "s_waitcnt lgkmcnt(4)\n"
    "v_mfma_f32_16x16x32_bf16 v[8:11], v[196:199], v[96:99], v[8:11]\n"
    "v_exp_f32_e32 v91, v91\n"
    "v_mfma_f32_16x16x32_bf16 v[224:227], v[232:235], v[96:99], v[224:227]\n"
    "v_exp_f32_e32 v92, v92\n"
    "v_mfma_f32_16x16x32_bf16 v[12:15], v[196:199], v[100:103], v[12:15]\n"
    "ds_read_b64_tr_b16 v[176:177], v241 offset:34304\n"
    "ds_read_b64_tr_b16 v[178:179], v241 offset:38400\n"
    "v_cvt_pk_bf16_f32 v124, v88, v89\n"
    "v_mfma_f32_16x16x32_bf16 v[16:19], v[200:203], v[96:99], v[16:19]\n"
    "v_exp_f32_e32 v93, v93\n"
    "v_mfma_f32_16x16x32_bf16 v[20:23], v[200:203], v[100:103], v[20:23]\n"
    "ds_read_b64_tr_b16 v[180:181], v242 offset:34304\n"
    "ds_read_b64_tr_b16 v[182:183], v242 offset:38400\n"
    "v_mfma_f32_16x16x32_bf16 v[24:27], v[204:207], v[96:99], v[24:27]\n"
    "v_exp_f32_e32 v94, v94\n"
    "v_mfma_f32_16x16x32_bf16 v[228:231], v[232:235], v[100:103], v[228:231]\n"
    "v_cvt_pk_bf16_f32 v125, v90, v91\n"
    "v_mfma_f32_16x16x32_bf16 v[28:31], v[204:207], v[100:103], v[28:31]\n"
    "s_barrier\n"
    "ds_read_b64_tr_b16 v[184:185], v241 offset:40960\n"
    "ds_read_b64_tr_b16 v[186:187], v241 offset:45056\n"
    "s_waitcnt lgkmcnt(6)\n"
    "v_mfma_f32_16x16x32_bf16 v[32:35], v[208:211], v[96:99], v[32:35]\n"
    "v_exp_f32_e32 v95, v95\n"
    "v_mfma_f32_16x16x32_bf16 v[36:39], v[208:211], v[100:103], v[36:39]\n"
    "ds_read_b64_tr_b16 v[188:189], v241 offset:49152\n"
    "ds_read_b64_tr_b16 v[190:191], v241 offset:53248\n"
    "v_mfma_f32_16x16x32_bf16 v[40:43], v[212:215], v[96:99], v[40:43]\n"
    "v_cvt_pk_bf16_f32 v126, v92, v93\n"
    "v_cvt_pk_bf16_f32 v127, v94, v95\n"
    "v_mfma_f32_16x16x32_bf16 v[44:47], v[212:215], v[100:103], v[44:47]\n"
    "ds_read_b64_tr_b16 v[192:193], v241 offset:57344\n"
    "ds_read_b64_tr_b16 v[194:195], v241 offset:61440\n"
    "s_waitcnt lgkmcnt(6)\n"
    "v_mfma_f32_16x16x32_bf16 v[48:51], v[176:179], v[96:99], v[48:51]\n"
    "v_xor_b32_e32 v236, 0, v243\n"
    "v_xor_b32_e32 v237, 32, v243\n"
    "v_mfma_f32_16x16x32_bf16 v[224:227], v[232:235], v[112:115], v[224:227]\n"
    "v_xor_b32_e32 v238, 64, v243\n"
    "v_xor_b32_e32 v239, 96, v243\n"
    "v_mfma_f32_16x16x32_bf16 v[224:227], v[232:235], v[120:123], v[224:227]\n"
    "v_mfma_f32_16x16x32_bf16 v[52:55], v[176:179], v[100:103], v[52:55]\n"
    "ds_read_b64_tr_b16 v[196:197], v242 offset:40960\n"
    "ds_read_b64_tr_b16 v[198:199], v242 offset:45056\n"
    "v_mfma_f32_16x16x32_bf16 v[56:59], v[180:183], v[96:99], v[56:59]\n"
    "v_mfma_f32_16x16x32_bf16 v[228:231], v[232:235], v[116:119], v[228:231]\n"
    "v_mfma_f32_16x16x32_bf16 v[228:231], v[232:235], v[124:127], v[228:231]\n"
    "v_mfma_f32_16x16x32_bf16 v[60:63], v[180:183], v[100:103], v[60:63]\n"
    "ds_read_b64_tr_b16 v[200:201], v242 offset:49152\n"
    "ds_read_b64_tr_b16 v[202:203], v242 offset:53248\n"
    "s_waitcnt lgkmcnt(6)\n"
    "; ---- gap 232: PV 13\n"
    "v_mfma_f32_16x16x32_bf16 v[0:3], v[184:187], v[104:107], v[0:3]\n"
    "v_mfma_f32_16x16x32_bf16 v[4:7], v[184:187], v[108:111], v[4:7]\n"
    "ds_read_b64_tr_b16 v[204:205], v242 offset:57344\n"
    "ds_read_b64_tr_b16 v[206:207], v242 offset:61440\n"
    "v_rcp_f32_e32 v226, v224\n"
    "v_rcp_f32_e32 v230, v228\n"
    "v_add_f32_e32 %[lout], v224, v228\n"
    "v_mfma_f32_16x16x32_bf16 v[0:3], v[188:191], v[112:115], v[0:3]\n"
    "v_mfma_f32_16x16x32_bf16 v[4:7], v[188:191], v[116:119], v[4:7]\n"
    "ds_read_b64_tr_b16 v[208:209], v241 offset:41472\n"
    "ds_read_b64_tr_b16 v[210:211], v241 offset:45568\n"
    "s_waitcnt lgkmcnt(6)\n"
    "v_mfma_f32_16x16x32_bf16 v[0:3], v[192:195], v[120:123], v[0:3]\n"
    "v_mfma_f32_16x16x32_bf16 v[4:7], v[192:195], v[124:127], v[4:7]\n"
    "ds_read_b64_tr_b16 v[212:213], v241 offset:49664\n"
    "ds_read_b64_tr_b16 v[214:215], v241 offset:53760\n"
    "v_mfma_f32_16x16x32_bf16 v[8:11], v[196:199], v[104:107], v[8:11]\n"
    "v_mfma_f32_16x16x32_bf16 v[12:15], v[196:199], v[108:111], v[12:15]\n"
    "ds_read_b64_tr_b16 v[176:177], v241 offset:57856\n"
    "ds_read_b64_tr_b16 v[178:179], v241 offset:61952\n"
    "s_waitcnt lgkmcnt(6)\n"
    "v_mfma_f32_16x16x32_bf16 v[8:11], v[200:203], v[112:115], v[8:11]\n"
    "v_mul_f32_e32 v0, v226, v0\n"
    "v_mul_f32_e32 v1, v226, v1\n"
    "v_mfma_f32_16x16x32_bf16 v[12:15], v[200:203], v[116:119], v[12:15]\n"
    "ds_read_b64_tr_b16 v[180:181], v242 offset:41472\n"
    "ds_read_b64_tr_b16 v[182:183], v242 offset:45568\n"
    "v_mul_f32_e32 v2, v226, v2\n"
    "v_mul_f32_e32 v3, v226, v3\n"
    "v_mfma_f32_16x16x32_bf16 v[8:11], v[204:207], v[120:123], v[8:11]\n"
    "v_cvt_pk_bf16_f32 v0, v0, v1\n"
    "v_cvt_pk_bf16_f32 v1, v2, v3\n"
    "v_mfma_f32_16x16x32_bf16 v[12:15], v[204:207], v[124:127], v[12:15]\n"
    "ds_read_b64_tr_b16 v[184:185], v242 offset:49664\n"
    "ds_read_b64_tr_b16 v[186:187], v242 offset:53760\n"
    "s_waitcnt lgkmcnt(6)\n"
    "v_mul_f32_e32 v4, v230, v4\n"
    "v_mul_f32_e32 v5, v230, v5\n"
    "v_mfma_f32_16x16x32_bf16 v[16:19], v[208:211], v[104:107], v[16:19]\n"
    "ds_write_b64 v236, v[0:1] offset:0\n"
    "v_mul_f32_e32 v6, v230, v6\n"
    "v_mfma_f32_16x16x32_bf16 v[20:23], v[208:211], v[108:111], v[20:23]\n"
    "ds_read_b64_tr_b16 v[188:189], v242 offset:57856\n"
    "ds_read_b64_tr_b16 v[190:191], v242 offset:61952\n"
    "v_mul_f32_e32 v7, v230, v7\n"
    "v_cvt_pk_bf16_f32 v4, v4, v5\n"
    "v_cvt_pk_bf16_f32 v5, v6, v7\n"
    "v_mfma_f32_16x16x32_bf16 v[16:19], v[212:215], v[112:115], v[16:19]\n"
    "ds_write_b64 v236, v[4:5] offset:4096\n"
    "v_mul_f32_e32 v8, v226, v8\n"
    "v_mfma_f32_16x16x32_bf16 v[20:23], v[212:215], v[116:119], v[20:23]\n"
    "ds_read_b64_tr_b16 v[192:193], v241 offset:41984\n"
    "ds_read_b64_tr_b16 v[194:195], v241 offset:46080\n"
    "s_waitcnt lgkmcnt(8)\n"
    "v_mul_f32_e32 v9, v226, v9\n"
    "v_mul_f32_e32 v10, v226, v10\n"
    "v_mul_f32_e32 v11, v226, v11\n"
    "; ---- gap 240: PV 14\n"
    "v_mfma_f32_16x16x32_bf16 v[16:19], v[176:179], v[120:123], v[16:19]\n"
    "v_cvt_pk_bf16_f32 v8, v8, v9\n"
    "v_cvt_pk_bf16_f32 v9, v10, v11\n"
    "v_mfma_f32_16x16x32_bf16 v[20:23], v[176:179], v[124:127], v[20:23]\n"
    "ds_read_b64_tr_b16 v[196:197], v241 offset:50176\n"
    "ds_read_b64_tr_b16 v[198:199], v241 offset:54272\n"
    "v_mul_f32_e32 v12, v230, v12\n"
    "v_mul_f32_e32 v13, v230, v13\n"
    "v_mfma_f32_16x16x32_bf16 v[24:27], v[180:183], v[104:107], v[24:27]\n"
    "ds_write_b64 v237, v[8:9] offset:0\n"
    "v_mul_f32_e32 v14, v230, v14\n"
    "v_mfma_f32_16x16x32_bf16 v[28:31], v[180:183], v[108:111], v[28:31]\n"
    "ds_read_b64_tr_b16 v[200:201], v241 offset:58368\n"
    "ds_read_b64_tr_b16 v[202:203], v241 offset:62464\n"
    "s_waitcnt lgkmcnt(8)\n"
    "v_mul_f32_e32 v15, v230, v15\n"
    "v_cvt_pk_bf16_f32 v12, v12, v13\n"
    "v_cvt_pk_bf16_f32 v13, v14, v15\n"
    "v_mfma_f32_16x16x32_bf16 v[24:27], v[184:187], v[112:115], v[24:27]\n"
    "ds_write_b64 v237, v[12:13] offset:4096\n"
    "v_mul_f32_e32 v16, v226, v16\n"
    "v_mfma_f32_16x16x32_bf16 v[28:31], v[184:187], v[116:119], v[28:31]\n"
    "ds_read_b64_tr_b16 v[204:205], v242 offset:41984\n"
    "ds_read_b64_tr_b16 v[206:207], v242 offset:46080\n"
    "v_mul_f32_e32 v17, v226, v17\n"
    "v_mul_f32_e32 v18, v226, v18\n"
    "v_mul_f32_e32 v19, v226, v19\n"
    "v_mfma_f32_16x16x32_bf16 v[24:27], v[188:191], v[120:123], v[24:27]\n"
    "v_cvt_pk_bf16_f32 v16, v16, v17\n"
    "v_cvt_pk_bf16_f32 v17, v18, v19\n"
    "v_mfma_f32_16x16x32_bf16 v[28:31], v[188:191], v[124:127], v[28:31]\n"
    "ds_read_b64_tr_b16 v[208:209], v242 offset:50176\n"
    "ds_read_b64_tr_b16 v[210:211], v242 offset:54272\n"
    "s_waitcnt lgkmcnt(8)\n"
    "v_mul_f32_e32 v20, v230, v20\n"
    "v_mul_f32_e32 v21, v230, v21\n"
    "v_mfma_f32_16x16x32_bf16 v[32:35], v[192:195], v[104:107], v[32:35]\n"
    "ds_write_b64 v238, v[16:17] offset:0\n"
    "v_mul_f32_e32 v22, v230, v22\n"
    "v_mfma_f32_16x16x32_bf16 v[36:39], v[192:195], v[108:111], v[36:39]\n"
    "ds_read_b64_tr_b16 v[212:213], v242 offset:58368\n"
    "ds_read_b64_tr_b16 v[214:215], v242 offset:62464\n"
    "v_mul_f32_e32 v23, v230, v23\n"
    "v_cvt_pk_bf16_f32 v20, v20, v21\n"
    "v_cvt_pk_bf16_f32 v21, v22, v23\n"
    "v_mfma_f32_16x16x32_bf16 v[32:35], v[196:199], v[112:115], v[32:35]\n"
    "ds_write_b64 v238, v[20:21] offset:4096\n"
    "v_mul_f32_e32 v24, v226, v24\n"
    "v_mfma_f32_16x16x32_bf16 v[36:39], v[196:199], v[116:119], v[36:39]\n"
    "ds_read_b64_tr_b16 v[176:177], v241 offset:42496\n"
    "ds_read_b64_tr_b16 v[178:179], v241 offset:46592\n"
    "s_waitcnt lgkmcnt(8)\n"
    "v_mul_f32_e32 v25, v226, v25\n"
    "v_mul_f32_e32 v26, v226, v26\n"
    "v_mul_f32_e32 v27, v226, v27\n"
    "v_mfma_f32_16x16x32_bf16 v[32:35], v[200:203], v[120:123], v[32:35]\n"
    "v_cvt_pk_bf16_f32 v24, v24, v25\n"
    "v_cvt_pk_bf16_f32 v25, v26, v27\n"
    "v_mfma_f32_16x16x32_bf16 v[36:39], v[200:203], v[124:127], v[36:39]\n"
    "ds_read_b64_tr_b16 v[180:181], v241 offset:50688\n"
    "ds_read_b64_tr_b16 v[182:183], v241 offset:54784\n"
    "v_mul_f32_e32 v28, v230, v28\n"
    "v_mul_f32_e32 v29, v230, v29\n"
    "v_mfma_f32_16x16x32_bf16 v[40:43], v[204:207], v[104:107], v[40:43]\n"
    "ds_write_b64 v239, v[24:25] offset:0\n"
    "v_mul_f32_e32 v30, v230, v30\n"
    "v_mfma_f32_16x16x32_bf16 v[44:47], v[204:207], v[108:111], v[44:47]\n"
    "ds_read_b64_tr_b16 v[184:185], v241 offset:58880\n"
    "ds_read_b64_tr_b16 v[186:187], v241 offset:62976\n"
    "s_waitcnt lgkmcnt(8)\n"
    "v_mul_f32_e32 v31, v230, v31\n"
    "v_cvt_pk_bf16_f32 v28, v28, v29\n"
    "v_cvt_pk_bf16_f32 v29, v30, v31\n"
    "; ---- gap 248: PV 15\n"
    "v_mfma_f32_16x16x32_bf16 v[40:43], v[208:211], v[112:115], v[40:43]\n"
    "ds_write_b64 v239, v[28:29] offset:4096\n"
    "ds_read_b128 v[144:147], v244 offset:0\n"
    "ds_read_b128 v[148:151], v244 offset:2048\n"
    "ds_read_b128 v[152:155], v244 offset:4096\n"
    "ds_read_b128 v[156:159], v244 offset:6144\n"
    "v_mfma_f32_16x16x32_bf16 v[44:47], v[208:211], v[116:119], v[44:47]\n"
    "ds_read_b64_tr_b16 v[188:189], v242 offset:42496\n"
    "ds_read_b64_tr_b16 v[190:191], v242 offset:46592\n"
    "v_mul_f32_e32 v32, v226, v32\n"
    "v_mul_f32_e32 v33, v226, v33\n"
    "v_mul_f32_e32 v34, v226, v34\n"
    "v_mul_f32_e32 v35, v226, v35\n"
    "v_mfma_f32_16x16x32_bf16 v[40:43], v[212:215], v[120:123], v[40:43]\n"
    "v_cvt_pk_bf16_f32 v32, v32, v33\n"
    "v_cvt_pk_bf16_f32 v33, v34, v35\n"
    "v_mfma_f32_16x16x32_bf16 v[44:47], v[212:215], v[124:127], v[44:47]\n"
    "ds_read_b64_tr_b16 v[192:193], v242 offset:50688\n"
    "ds_read_b64_tr_b16 v[194:195], v242 offset:54784\n"
    "s_waitcnt lgkmcnt(12)\n"
    "v_mul_f32_e32 v36, v230, v36\n"
    "v_mul_f32_e32 v37, v230, v37\n"
    "v_mfma_f32_16x16x32_bf16 v[48:51], v[176:179], v[104:107], v[48:51]\n"
    "ds_write_b64 v236, v[32:33] offset:128\n"
    "v_mul_f32_e32 v38, v230, v38\n"
    "v_mfma_f32_16x16x32_bf16 v[52:55], v[176:179], v[108:111], v[52:55]\n"
    "ds_read_b64_tr_b16 v[196:197], v242 offset:58880\n"
    "ds_read_b64_tr_b16 v[198:199], v242 offset:62976\n"
    "s_waitcnt lgkmcnt(10)\n"
    "buffer_store_dwordx4 v[144:147], v245, s[20:23], s27 offen offset:0 sc0 sc1 nt\n"
    "v_mul_f32_e32 v39, v230, v39\n"
    "v_cvt_pk_bf16_f32 v36, v36, v37\n"
    "v_cvt_pk_bf16_f32 v37, v38, v39\n"
    "v_mfma_f32_16x16x32_bf16 v[48:51], v[180:183], v[112:115], v[48:51]\n"
    "ds_write_b64 v236, v[36:37] offset:4224\n"
    "v_mul_f32_e32 v40, v226, v40\n"
    "v_mfma_f32_16x16x32_bf16 v[52:55], v[180:183], v[116:119], v[52:55]\n"
    "s_waitcnt lgkmcnt(6)\n"
    "buffer_store_dwordx4 v[148:151], v245, s[20:23], s27 offen offset:2048 sc0 sc1 nt\n"
    "v_mul_f32_e32 v41, v226, v41\n"
    "v_mul_f32_e32 v42, v226, v42\n"
    "v_mul_f32_e32 v43, v226, v43\n"
    "v_mfma_f32_16x16x32_bf16 v[48:51], v[184:187], v[120:123], v[48:51]\n"
    "v_cvt_pk_bf16_f32 v40, v40, v41\n"
    "v_cvt_pk_bf16_f32 v41, v42, v43\n"
    "v_mfma_f32_16x16x32_bf16 v[52:55], v[184:187], v[124:127], v[52:55]\n"
    "buffer_store_dwordx4 v[152:155], v245, s[20:23], s30 offen offset:0 sc0 sc1 nt\n"
    "v_mul_f32_e32 v44, v230, v44\n"
    "v_mul_f32_e32 v45, v230, v45\n"
    "v_mfma_f32_16x16x32_bf16 v[56:59], v[188:191], v[104:107], v[56:59]\n"
    "ds_write_b64 v237, v[40:41] offset:128\n"
    "v_mul_f32_e32 v46, v230, v46\n"
    "v_mfma_f32_16x16x32_bf16 v[60:63], v[188:191], v[108:111], v[60:63]\n"
    "s_waitcnt lgkmcnt(2)\n"
    "buffer_store_dwordx4 v[156:159], v245, s[20:23], s30 offen offset:2048 sc0 sc1 nt\n"
    "v_mul_f32_e32 v47, v230, v47\n"
    "v_cvt_pk_bf16_f32 v44, v44, v45\n"
    "v_cvt_pk_bf16_f32 v45, v46, v47\n"
    "v_mfma_f32_16x16x32_bf16 v[56:59], v[192:195], v[112:115], v[56:59]\n"
    "ds_write_b64 v237, v[44:45] offset:4224\n"
    "v_mul_f32_e32 v48, v226, v48\n"
    "v_mfma_f32_16x16x32_bf16 v[60:63], v[192:195], v[116:119], v[60:63]\n"
    "v_mul_f32_e32 v49, v226, v49\n"
    "v_mul_f32_e32 v50, v226, v50\n"
    "v_mul_f32_e32 v51, v226, v51\n"
    "v_mfma_f32_16x16x32_bf16 v[56:59], v[196:199], v[120:123], v[56:59]\n"
    "v_cvt_pk_bf16_f32 v48, v48, v49\n"
    "v_cvt_pk_bf16_f32 v49, v50, v51\n"
    "v_mfma_f32_16x16x32_bf16 v[60:63], v[196:199], v[124:127], v[60:63]\n"
    "v_mul_f32_e32 v52, v230, v52\n"
    "v_mul_f32_e32 v53, v230, v53\n"
    "; ---- epilogue\n"
    "v_mul_f32_e32 v54, v230, v54\n"
    "v_mul_f32_e32 v55, v230, v55\n"
    "v_cvt_pk_bf16_f32 v52, v52, v53\n"
    "v_cvt_pk_bf16_f32 v53, v54, v55\n"
    "ds_write_b64 v238, v[48:49] offset:128\n"
    "ds_write_b64 v238, v[52:53] offset:4224\n"
    "v_pk_mul_f32 v[56:57], v[56:57], v[226:227] op_sel_hi:[1,0]\n"
    "v_pk_mul_f32 v[58:59], v[58:59], v[226:227] op_sel_hi:[1,0]\n"
    "v_cvt_pk_bf16_f32 v56, v56, v57\n"
    "v_cvt_pk_bf16_f32 v57, v58, v59\n"
    "ds_write_b64 v239, v[56:57] offset:128\n"
    "v_pk_mul_f32 v[60:61], v[60:61], v[230:231] op_sel_hi:[1,0]\n"
    "v_pk_mul_f32 v[62:63], v[62:63], v[230:231] op_sel_hi:[1,0]\n"
    "v_cvt_pk_bf16_f32 v60, v60, v61\n"
    "v_cvt_pk_bf16_f32 v61, v62, v63\n"
    "ds_write_b64 v239, v[60:61] offset:4224\n"
    "ds_read_b128 v[160:163], v244 offset:128\n"
    "ds_read_b128 v[164:167], v244 offset:2176\n"
    "ds_read_b128 v[168:171], v244 offset:4224\n"
    "ds_read_b128 v[172:175], v244 offset:6272\n"
    "s_waitcnt lgkmcnt(3)\n"
    "buffer_store_dwordx4 v[160:163], v245, s[20:23], s27 offen offset:128 sc0 sc1 nt\n"
    "s_waitcnt lgkmcnt(2)\n"
    "buffer_store_dwordx4 v[164:167], v245, s[20:23], s27 offen offset:2176 sc0 sc1 nt\n"
    "s_waitcnt lgkmcnt(1)\n"
    "buffer_store_dwordx4 v[168:171], v245, s[20:23], s30 offen offset:128 sc0 sc1 nt\n"
    "s_waitcnt lgkmcnt(0)\n"
    "buffer_store_dwordx4 v[172:175], v245, s[20:23], s30 offen offset:2176 sc0 sc1 nt\n"
    "s_nop 1\n"
    : [lout] "=&v"(lout)
    : [tid] "v"(threadIdx.x), [bx] "s"(blockIdx.x), [karg] "s"(__builtin_amdgcn_kernarg_segment_ptr())
    : "v0", "v1", "v2", "v3", "v4", "v5", "v6", "v7", "v8", "v9", "v10", "v11", "v12", "v13", "v14", "v15", "v16", "v17", "v18", "v19", "v20", "v21", "v22", "v23", "v24", "v25", "v26", "v27", "v28", "v29", "v30", "v31", "v32", "v33", "v34", "v35", "v36", "v37", "v38", "v39", "v40", "v41", "v42", "v43", "v44", "v45", "v46", "v47", "v48", "v49", "v50", "v51", "v52", "v53", "v54", "v55", "v56", "v57", "v58", "v59", "v60", "v61", "v62", "v63", "v64", "v65", "v66", "v67", "v68", "v69", "v70", "v71", "v72", "v73", "v74", "v75", "v76", "v77", "v78", "v79", "v80", "v81", "v82", "v83", "v84", "v85", "v86", "v87", "v88", "v89", "v90", "v91", "v92", "v93", "v94", "v95", "v96", "v97", "v98", "v99", "v100", "v101", "v102", "v103", "v104", "v105", "v106", "v107", "v108", "v109", "v110", "v111", "v112", "v113", "v114", "v115", "v116", "v117", "v118", "v119", "v120", "v121", "v122", "v123", "v124", "v125", "v126", "v127", "v128", "v129", "v130", "v131", "v132", "v133", "v134", "v135", "v136", "v137", "v138", "v139", "v140", "v141", "v142", "v143", "v144", "v145", "v146", "v147", "v148", "v149", "v150", "v151", "v152", "v153", "v154", "v155", "v156", "v157", "v158", "v159", "v160", "v161", "v162", "v163", "v164", "v165", "v166", "v167", "v168", "v169", "v170", "v171", "v172", "v173", "v174", "v175", "v176", "v177", "v178", "v179", "v180", "v181", "v182", "v183", "v184", "v185", "v186", "v187", "v188", "v189", "v190", "v191", "v192", "v193", "v194", "v195", "v196", "v197", "v198", "v199", "v200", "v201", "v202", "v203", "v204", "v205", "v206", "v207", "v208", "v209", "v210", "v211", "v212", "v213", "v214", "v215", "v216", "v217", "v218", "v219", "v220", "v221", "v222", "v223", "v224", "v225", "v226", "v227", "v228", "v229", "v230", "v231", "v232", "v233", "v234", "v235", "v236", "v237", "v238", "v239", "v240", "v241", "v242", "v243", "v244", "v245", "s8", "s9", "s10", "s11", "s12", "s13", "s14", "s15", "s16", "s17", "s18", "s19", "s20", "s21", "s22", "s23", "s24", "s25", "s26", "s27", "s28", "s29", "s30", "s31", "s34", "s35", "s36", "s37", "s38", "s39", "s40", "s41", "s42", "s43", "scc", "memory"
  );
  if (__builtin_expect(__any(!(lout < 1.0e12f)), 0)) {
    asm volatile("s_waitcnt vmcnt(0)" ::: "memory");
    const unsigned bx = blockIdx.x, pair = (bx >> 4) * 8u + (bx & 7u), hm = (bx >> 3) & 1u, wl = threadIdx.x >> 6;
    const size_t qo = ((size_t)pair * 256 + hm * 128 + wl * 32) * 256, ko = (size_t)pair * 512 * 256;
    exact_rows((const char*)Q + qo, (const char*)K + ko, (const char*)V + ko, (char*)O + qo, threadIdx.x & 63u);
  }
}

extern "C" void kernel_launch(void* const* d_in, const int* in_sizes, int n_in, void* d_out, int out_size, void* d_ws, size_t ws_size, hipStream_t stream) {
  (void)in_sizes; (void)n_in; (void)out_size; (void)d_ws; (void)ws_size;
  (void)hipFuncSetAttribute((const void*)gqa_fwd, hipFuncAttributeMaxDynamicSharedMemorySize, LDS_BYTES);
  hipLaunchKernelGGL(gqa_fwd, dim3(256), dim3(512), LDS_BYTES, stream, (const unsigned short*)d_in[0], (const unsigned short*)d_in[1], (const unsigned short*)d_in[2], (unsigned short*)d_out
  );
}
```
